```python
import torch

def _softmax(x, axis=-1):
    return torch.softmax(x, dim=axis)

def _gelu(x):
    return torch.nn.functional.gelu(x, approximate='tanh')

def _silu(x):
    return torch.nn.functional.silu(x)

def _sigmoid(x):
    return torch.sigmoid(x)

def _layernorm(x, axis=-1, eps=1e-05):
    m = x.mean(dim=axis, keepdim=True)
    v = x.var(dim=axis, keepdim=True, unbiased=False)
    return (x - m) / torch.sqrt(v + eps)

def _scatter_add(tgt, idx, src, axis=0):
    return tgt.clone().index_add_(axis, idx, src)

def _segment_sum(src, seg, n):
    out = torch.zeros((n,) + src.shape[1:], dtype=src.dtype)
    return out.index_add_(0, seg, src)

def _pad(x, pw):
    flat = tuple((v for pair in reversed(pw) for v in pair))
    return torch.nn.functional.pad(x, flat)
kernel_name = 'adaLN_cond__mha_block_cached__mixer_gmlp__prompt'

def setup_inputs():

    _w = lambda a: (lambda t: t.to(torch.bfloat16) if t.is_floating_point() else t)(torch.as_tensor(a))

    def _A_setup():
        B, Sq, D = (1, 2048, 512)
        H, Sk, Dh = (32, 2048, 128)
        return {'x': _w((lambda *s: torch.randn(*s, dtype=torch.bfloat16) * 0.05)(B, Sq, D)), 'k_cache': _w((lambda *s: torch.randn(*s, dtype=torch.bfloat16) * 3.2)(B, H, Sk, Dh)), 'v_cache': _w((lambda *s: torch.randn(*s, dtype=torch.bfloat16) * 0.4)(B, H, Sk, Dh)), 'wq': _w((lambda *s: torch.randn(*s, dtype=torch.bfloat16) * 3.2)(D, H * Dh) * D ** (-0.5)), 'wo': _w((lambda *s: torch.randn(*s, dtype=torch.bfloat16) * 0.4)(H * Dh, D) * (H * Dh) ** (-0.5))}
    _A_in = _A_setup()

    def _B_setup():
        B, S, D, F = (1, 2048, 512, 1792)
        Fh = F // 2
        return {'x': _w((lambda *s: torch.randn(*s, dtype=torch.bfloat16) * 0.05)(B, S, D)), 'w_in': _w((lambda *s: torch.randn(*s, dtype=torch.bfloat16) * 0.8)(D, 2 * Fh)), 'w_sp': _w(0.01 * (lambda *s: torch.randn(*s, dtype=torch.bfloat16) * 0.8)(S, S)), 'b_sp': _w(1.0 + 0.01 * (lambda *s: torch.randn(*s, dtype=torch.bfloat16) * 0.05)(S)), 'w_out': _w((lambda *s: torch.randn(*s, dtype=torch.bfloat16) * 0.05)(Fh, D))}
    _B_in = _B_setup()
    return {'A_x': _A_in['x'], 'A_k_cache': _A_in['k_cache'], 'A_v_cache': _A_in['v_cache'], 'A_wq': _A_in['wq'], 'A_wo': _A_in['wo'], 'B_x': _B_in['x'], 'B_w_in': _B_in['w_in'], 'B_w_sp': _B_in['w_sp'], 'B_b_sp': _B_in['b_sp'], 'B_w_out': _B_in['w_out'], 'c': _w((lambda *s: torch.randn(*s, dtype=torch.bfloat16) * 0.1)(1, 512) * 8.0), 'w_mod': _w((lambda *s: torch.randn(*s, dtype=torch.bfloat16) * 0.1)(512, 6 * 512) * 512 ** (-0.5) * 8.0)}

def reference(A_x, A_k_cache, A_v_cache, A_wq, A_wo, B_x, B_w_in, B_w_sp, B_b_sp, B_w_out, c, w_mod):

    def _A_ref(x, k_cache, v_cache, wq, wo):
        B, Sq, D = x.shape
        H, Dh = (32, 128)
        q = (lambda x, *d: x.permute(*d))((x @ wq).reshape(B, Sq, H, Dh), 0, 2, 1, 3)
        scores = torch.einsum('bhqd,bhkd->bhqk', q, k_cache) * Dh ** (-0.5)
        p = _softmax(scores, axis=-1)
        o = torch.einsum('bhqk,bhkd->bhqd', p, v_cache)
        return (lambda x, *d: x.permute(*d))(o, 0, 2, 1, 3).reshape(B, Sq, H * Dh) @ wo

    def _B_ref(x, w_in, w_sp, b_sp, w_out):
        B, S, D = x.shape
        F = w_in.shape[-1]
        Fh = F // 2
        z = _gelu(torch.einsum('bsd,df->bsf', x, w_in))
        z1 = z[..., :Fh].clone()
        z2 = _layernorm(z[..., Fh:], axis=-1)
        g = torch.einsum('ts,bsf->btf', w_sp, z2) + b_sp[None, :, None]
        return torch.einsum('bsf,fd->bsd', z1 * g, w_out)
    x = A_x
    mod = (_silu(c) @ w_mod).reshape(c.shape[0], 6, 1, -1)
    sa, ca, ga, sb, cb, gb = (mod[:, 0].clone(), mod[:, 1].clone(), mod[:, 2].clone(), mod[:, 3].clone(), mod[:, 4].clone(), mod[:, 5].clone())
    h = _layernorm(x, axis=-1) * (1 + ca) + sa
    x = x + ga * _A_ref(h, A_k_cache, A_v_cache, A_wq, A_wo)
    h = _layernorm(x, axis=-1) * (1 + cb) + sb
    x = x + gb * _B_ref(h, B_w_in, B_w_sp, B_b_sp, B_w_out)
    return x
```

```cpp
#include <hip/hip_runtime.h>
#include <hip/hip_bf16.h>
#include <stdint.h>


typedef unsigned short u16;
typedef short bf16x8 __attribute__((ext_vector_type(8)));
typedef short s16x4 __attribute__((ext_vector_type(4)));
typedef float f32x16 __attribute__((ext_vector_type(16)));
typedef float f32x4 __attribute__((ext_vector_type(4)));
typedef unsigned u32x4 __attribute__((ext_vector_type(4)));

constexpr int SEQ = 2048, DM = 512, NH = 32, HD = 128, SK = 2048, FH = 896, FF = 1792;
constexpr float LN_EPS = 1e-5f;

__device__ __forceinline__ u16 f2bf(float f) {
  unsigned u = __float_as_uint(f);
  u += 0x7fffu + ((u >> 16) & 1u);
  return (u16)(u >> 16);
}
__device__ __forceinline__ float bf2f(u16 b) { return __uint_as_float(((unsigned)b) << 16); }
__device__ __forceinline__ float rbf(float f) { return bf2f(f2bf(f)); }
__device__ __forceinline__ int crow(int r, int hi) { return (r & 3) + 8 * (r >> 2) + 4 * hi; }
__device__ __forceinline__ unsigned cvtpk(float lo, float hi) {
  unsigned r;
  asm volatile("v_cvt_pk_bf16_f32 %0, %1, %2" : "=v"(r) : "v"(lo), "v"(hi));
  return r;
}
#define SBAR() __builtin_amdgcn_sched_barrier(0)

__global__ __launch_bounds__(256) void k_mod(const u16* __restrict__ c, const u16* __restrict__ wmod,
                                             float* __restrict__ mod) {
  __shared__ float red[4][64];
  __shared__ float sc[DM];
  const int tid = threadIdx.x;
  for (int i = tid; i < DM; i += 256) {
    float x = bf2f(c[i]);
    sc[i] = rbf(x / (1.f + __expf(-x)));
  }
  __syncthreads();
  const int j = blockIdx.x * 64 + (tid & 63);
  const int kq = tid >> 6;
  float acc = 0.f;
#pragma unroll 8
  for (int k = kq * 128; k < kq * 128 + 128; ++k) acc += sc[k] * bf2f(wmod[(size_t)k * (6 * DM) + j]);
  red[kq][tid & 63] = acc;
  __syncthreads();
  if (tid < 64) mod[j] = rbf(red[0][tid] + red[1][tid] + red[2][tid] + red[3][tid]);
}

__device__ __forceinline__ float wave_sum(float v) {
#pragma unroll
  for (int o = 32; o >= 1; o >>= 1) v += __shfl_xor(v, o, 64);
  return v;
}

__global__ __launch_bounds__(256) void k_ln_mod(const u16* __restrict__ x, const float* __restrict__ mod,
                                                u16* __restrict__ h1) {
  const int wid = threadIdx.x >> 6, lane = threadIdx.x & 63;
  const int row = blockIdx.x * 4 + wid;
  const bf16x8 raw = *reinterpret_cast<const bf16x8*>(x + (size_t)row * DM + lane * 8);
  float v[8];
  float s = 0.f;
#pragma unroll
  for (int i = 0; i < 8; ++i) { v[i] = bf2f((u16)raw[i]); s += v[i]; }
  s = wave_sum(s);
  const float mean = s * (1.f / DM);
  float q = 0.f;
#pragma unroll
  for (int i = 0; i < 8; ++i) { float d = v[i] - mean; q += d * d; }
  q = wave_sum(q);
  const float mb = rbf(mean), vb = rbf(q * (1.f / DM));
  const float den = rbf(sqrtf(rbf(vb + LN_EPS)));
  bf16x8 o;
#pragma unroll
  for (int i = 0; i < 8; ++i) {
    const int cidx = lane * 8 + i;
    const float sa = mod[cidx], ca = mod[DM + cidx];
    float n = rbf(rbf(v[i] - mb) / den);
    float h = rbf(rbf(n * rbf(1.f + ca)) + sa);
    o[i] = (short)f2bf(h);
  }
  *reinterpret_cast<bf16x8*>(h1 + (size_t)row * DM + lane * 8) = o;
}

__device__ __forceinline__ int a_off(int row, int colB) { return row * 128 + (colB ^ (((row >> 1) & 7) << 4)); }
__device__ __forceinline__ int v_st(int k, int c) {
  const int kk = (k & ~0xC) | ((k & 4) << 1) | ((k & 8) >> 1);
  return ((kk >> 3) * 4 + (c >> 5)) * 512 + ((kk & 7) * 32 + (c & 31)) * 2;
}
__device__ __forceinline__ int v_rd_base(int lane) {
  return ((lane & 3) << 3) | (((lane >> 2) & 3) << 6) | (((lane >> 4) & 1) << 5) | (((lane >> 5) & 1) << 8);
}
constexpr int v_rd_off(int d0, int ks, int half) { return d0 * 512 + ks * 4096 + half * 2048; }
template <int OFF>
__device__ __forceinline__ s16x4 tr_read(int vb) {
  s16x4 r;
  asm volatile("ds_read_b64_tr_b16 %0, %1 offset:%2" : "=&v"(r) : "v"(vb), "i"(OFF) : "memory");
  return r;
}

enum { EPI_BF16 = 0, EPI_F32PART = 1, EPI_GELU = 2, EPI_GATE = 3, EPI_FINAL = 4 };

struct GemmArgs {
  const u16* A; const u16* B;
  void* C; void* C2;
  const u16* aux0; const u16* aux1; const float* mod;
  int lda, ldb, ldc, K;
};

template <int BM, int WM, int WN, int EPI>
__global__ __launch_bounds__(256) void k_gemm(GemmArgs g) {
  constexpr int BN = 128, BK = 64;
  constexpr int TM = BM / WM / 32, TN = BN / WN / 32;
  static_assert(WM * WN == 4, "4 waves");
  __shared__ __attribute__((aligned(16))) char lds[2 * (BM * 128 + 16384)];
  char* As = lds;
  char* Bs = lds + 2 * BM * 128;
  const int tid = threadIdx.x, wid = tid >> 6, lane = tid & 63, r32 = lane & 31, hi = lane >> 5;
  const int wm = wid / WN, wn = wid % WN;
  const int bm = blockIdx.y * BM, bn = blockIdx.x * BN;
  const int kbase = blockIdx.z * g.K;
  const u16* Ab = g.A + (size_t)bm * g.lda + kbase;
  const u16* Bb = g.B + (size_t)kbase * g.ldb + bn;
  constexpr int ACH = BM * 8 / 256;
  bf16x8 ra[ACH], rb[4];
  auto gload = [&](int kt) {
#pragma unroll
    for (int i = 0; i < ACH; ++i) {
      const int id = tid + i * 256, row = id >> 3, c16 = id & 7;
      ra[i] = *reinterpret_cast<const bf16x8*>(Ab + (size_t)row * g.lda + kt * BK + c16 * 8);
    }
#pragma unroll
    for (int i = 0; i < 4; ++i) {
      const int id = tid + i * 256, k = id >> 4, c = (id & 15) * 8;
      rb[i] = *reinterpret_cast<const bf16x8*>(Bb + (size_t)(kt * BK + k) * g.ldb + c);
    }
  };
  auto swrite = [&](int b) {
#pragma unroll
    for (int i = 0; i < ACH; ++i) {
      const int id = tid + i * 256, row = id >> 3, c16 = id & 7;
      *reinterpret_cast<bf16x8*>(As + b * BM * 128 + a_off(row, c16 * 16)) = ra[i];
    }
#pragma unroll
    for (int i = 0; i < 4; ++i) {
      const int id = tid + i * 256, k = id >> 4, c = (id & 15) * 8;
      *reinterpret_cast<bf16x8*>(Bs + b * 16384 + v_st(k, c)) = rb[i];
    }
  };
  f32x16 acc[TM][TN];
#pragma unroll
  for (int i = 0; i < TM; ++i)
#pragma unroll
    for (int j = 0; j < TN; ++j) acc[i][j] = f32x16{};

  const int nk = g.K / BK;
  gload(0);
  swrite(0);
  __syncthreads();
  const int vbB = (int)(uintptr_t)Bs + v_rd_base(lane) + wn * TN * 512;
  for (int kt = 0; kt < nk; ++kt) {
    const int b = kt & 1;
    if (kt + 1 < nk) gload(kt + 1);
    const char* Ac = As + b * BM * 128;
    const int vb = vbB + b * 16384;
#pragma unroll
    for (int ks = 0; ks < 4; ++ks) {
      bf16x8 af[TM];
#pragma unroll
      for (int i = 0; i < TM; ++i)
        af[i] = *reinterpret_cast<const bf16x8*>(Ac + a_off(wm * (BM / WM) + i * 32 + r32, (ks * 16 + hi * 8) * 2));
      s16x4 bl[TN], bh[TN];
#pragma unroll
      for (int j = 0; j < TN; ++j) {
        if (ks == 0) { if (j == 0) { bl[j] = tr_read<v_rd_off(0, 0, 0)>(vb); bh[j] = tr_read<v_rd_off(0, 0, 1)>(vb); }
                       else if (j == 1) { bl[j] = tr_read<v_rd_off(1, 0, 0)>(vb); bh[j] = tr_read<v_rd_off(1, 0, 1)>(vb); }
                       else if (j == 2) { bl[j] = tr_read<v_rd_off(2, 0, 0)>(vb); bh[j] = tr_read<v_rd_off(2, 0, 1)>(vb); }
                       else { bl[j] = tr_read<v_rd_off(3, 0, 0)>(vb); bh[j] = tr_read<v_rd_off(3, 0, 1)>(vb); } }
        else if (ks == 1) { if (j == 0) { bl[j] = tr_read<v_rd_off(0, 1, 0)>(vb); bh[j] = tr_read<v_rd_off(0, 1, 1)>(vb); }
                       else if (j == 1) { bl[j] = tr_read<v_rd_off(1, 1, 0)>(vb); bh[j] = tr_read<v_rd_off(1, 1, 1)>(vb); }
                       else if (j == 2) { bl[j] = tr_read<v_rd_off(2, 1, 0)>(vb); bh[j] = tr_read<v_rd_off(2, 1, 1)>(vb); }
                       else { bl[j] = tr_read<v_rd_off(3, 1, 0)>(vb); bh[j] = tr_read<v_rd_off(3, 1, 1)>(vb); } }
        else if (ks == 2) { if (j == 0) { bl[j] = tr_read<v_rd_off(0, 2, 0)>(vb); bh[j] = tr_read<v_rd_off(0, 2, 1)>(vb); }
                       else if (j == 1) { bl[j] = tr_read<v_rd_off(1, 2, 0)>(vb); bh[j] = tr_read<v_rd_off(1, 2, 1)>(vb); }
                       else if (j == 2) { bl[j] = tr_read<v_rd_off(2, 2, 0)>(vb); bh[j] = tr_read<v_rd_off(2, 2, 1)>(vb); }
                       else { bl[j] = tr_read<v_rd_off(3, 2, 0)>(vb); bh[j] = tr_read<v_rd_off(3, 2, 1)>(vb); } }
        else { if (j == 0) { bl[j] = tr_read<v_rd_off(0, 3, 0)>(vb); bh[j] = tr_read<v_rd_off(0, 3, 1)>(vb); }
                       else if (j == 1) { bl[j] = tr_read<v_rd_off(1, 3, 0)>(vb); bh[j] = tr_read<v_rd_off(1, 3, 1)>(vb); }
                       else if (j == 2) { bl[j] = tr_read<v_rd_off(2, 3, 0)>(vb); bh[j] = tr_read<v_rd_off(2, 3, 1)>(vb); }
                       else { bl[j] = tr_read<v_rd_off(3, 3, 0)>(vb); bh[j] = tr_read<v_rd_off(3, 3, 1)>(vb); } }
      }
      asm volatile("s_waitcnt lgkmcnt(0)" ::: "memory");
      SBAR();
#pragma unroll
      for (int j = 0; j < TN; ++j) {
        const bf16x8 bfr = {bl[j][0], bl[j][1], bl[j][2], bl[j][3], bh[j][0], bh[j][1], bh[j][2], bh[j][3]};
#pragma unroll
        for (int i = 0; i < TM; ++i) acc[i][j] = __builtin_amdgcn_mfma_f32_32x32x16_bf16(af[i], bfr, acc[i][j], 0, 0, 0);
      }
    }
    if (kt + 1 < nk) swrite(b ^ 1);
    __syncthreads();
  }
#pragma unroll
  for (int i = 0; i < TM; ++i)
#pragma unroll
    for (int j = 0; j < TN; ++j) {
      const int col = bn + wn * (BN / WN) + j * 32 + r32;
#pragma unroll
      for (int r = 0; r < 16; ++r) {
        const int row = bm + wm * (BM / WM) + i * 32 + crow(r, hi);
        const float v = acc[i][j][r];
        if constexpr (EPI == EPI_BF16) {
          ((u16*)g.C)[(size_t)row * g.ldc + col] = f2bf(v);
        } else if constexpr (EPI == EPI_F32PART) {
          ((float*)g.C)[((size_t)blockIdx.z * SEQ + row) * g.ldc + col] = v;
        } else if constexpr (EPI == EPI_GELU) {
          const float xb = rbf(v);
          const float t = 0.7978845608028654f * (xb + 0.044715f * xb * xb * xb);
          const float ge = 0.5f * xb * (1.f + tanhf(t));
          if (col < FH) ((u16*)g.C)[(size_t)row * FH + col] = f2bf(ge);
          else ((u16*)g.C2)[(size_t)row * FH + (col - FH)] = f2bf(ge);
        } else if constexpr (EPI == EPI_GATE) {
          const float gg = rbf(rbf(v) + bf2f(g.aux0[row]));
          const float z1 = bf2f(g.aux1[(size_t)row * FH + col]);
          ((u16*)g.C)[(size_t)row * FH + col] = f2bf(z1 * gg);
        } else {
          const float o = rbf(v);
          const float gb = g.mod[5 * DM + col];
          const float x1 = bf2f(g.aux0[(size_t)row * DM + col]);
          ((u16*)g.C)[(size_t)row * DM + col] = f2bf(x1 + rbf(gb * o));
        }
      }
    }
}

constexpr int SPLITA = 4;
__global__ __launch_bounds__(256) void k_reduce_ln(const float* __restrict__ part, const u16* __restrict__ x,
                                                   const float* __restrict__ mod, u16* __restrict__ x1,
                                                   u16* __restrict__ h2) {
  const int wid = threadIdx.x >> 6, lane = threadIdx.x & 63;
  const int row = blockIdx.x * 4 + wid;
  const bf16x8 raw = *reinterpret_cast<const bf16x8*>(x + (size_t)row * DM + lane * 8);
  float a[8];
#pragma unroll
  for (int i = 0; i < 8; ++i) a[i] = 0.f;
#pragma unroll
  for (int s = 0; s < SPLITA; ++s) {
    const f32x4* p = reinterpret_cast<const f32x4*>(part + ((size_t)s * SEQ + row) * DM + lane * 8);
    f32x4 p0 = p[0], p1 = p[1];
#pragma unroll
    for (int i = 0; i < 4; ++i) { a[i] += p0[i]; a[4 + i] += p1[i]; }
  }
  float v[8];
  float s = 0.f;
#pragma unroll
  for (int i = 0; i < 8; ++i) {
    const int cidx = lane * 8 + i;
    const float ga = mod[2 * DM + cidx];
    v[i] = rbf(bf2f((u16)raw[i]) + rbf(ga * rbf(a[i])));
    s += v[i];
  }
  s = wave_sum(s);
  const float mean = s * (1.f / DM);
  float q = 0.f;
#pragma unroll
  for (int i = 0; i < 8; ++i) { float d = v[i] - mean; q += d * d; }
  q = wave_sum(q);
  const float mb = rbf(mean), vb = rbf(q * (1.f / DM));
  const float den = rbf(sqrtf(rbf(vb + LN_EPS)));
  bf16x8 o, ox;
#pragma unroll
  for (int i = 0; i < 8; ++i) {
    const int cidx = lane * 8 + i;
    const float sb = mod[3 * DM + cidx], cb = mod[4 * DM + cidx];
    float n = rbf(rbf(v[i] - mb) / den);
    float h = rbf(rbf(n * rbf(1.f + cb)) + sb);
    o[i] = (short)f2bf(h);
    ox[i] = (short)f2bf(v[i]);
  }
  *reinterpret_cast<bf16x8*>(h2 + (size_t)row * DM + lane * 8) = o;
  *reinterpret_cast<bf16x8*>(x1 + (size_t)row * DM + lane * 8) = ox;
}

__global__ __launch_bounds__(256) void k_ln_z2(const u16* __restrict__ z2, u16* __restrict__ z2n) {
  const int wid = threadIdx.x >> 6, lane = threadIdx.x & 63;
  const int row = blockIdx.x * 4 + wid;
  const unsigned* p = reinterpret_cast<const unsigned*>(z2 + (size_t)row * FH);
  float v[14];
  float s = 0.f;
#pragma unroll
  for (int i = 0; i < 7; ++i) {
    unsigned w = p[i * 64 + lane];
    v[2 * i] = bf2f((u16)(w & 0xffff));
    v[2 * i + 1] = bf2f((u16)(w >> 16));
    s += v[2 * i] + v[2 * i + 1];
  }
  s = wave_sum(s);
  const float mean = s * (1.f / FH);
  float q = 0.f;
#pragma unroll
  for (int i = 0; i < 14; ++i) { float d = v[i] - mean; q += d * d; }
  q = wave_sum(q);
  const float mb = rbf(mean), vb = rbf(q * (1.f / FH));
  const float den = rbf(sqrtf(rbf(vb + LN_EPS)));
  unsigned* o = reinterpret_cast<unsigned*>(z2n + (size_t)row * FH);
#pragma unroll
  for (int i = 0; i < 7; ++i) {
    const u16 a = f2bf(rbf(v[2 * i] - mb) / den), b = f2bf(rbf(v[2 * i + 1] - mb) / den);
    o[i * 64 + lane] = (unsigned)a | ((unsigned)b << 16);
  }
}

constexpr int NW = 8, QBLK = 32, KVBLK = 64;
constexpr float SCALE = 0.088388347648318440f;
constexpr float THR = 8.f;
constexpr int LDQ = NH * HD, LDK = HD, LDO = NH * HD;
constexpr size_t SHM_V = KVBLK * HD * 2, SHM_K = KVBLK * HD * 2, SHM_ATTN = 2 * SHM_V + 2 * SHM_K + NW * 64 * 4;
#define KSWZ(row, colB) ((row) * 256 + ((colB) ^ (((row) & 7) << 4)))

__device__ __forceinline__ void partialSM(f32x16& p0, f32x16& p1, float& m_reg, float& mn, float& alpha) {
  constexpr float C = SCALE * 1.4426950408889634f;
  float pmax = p0[0];
  for (int r = 1; r < 16; ++r) pmax = fmaxf(pmax, p0[r]);
  for (int r = 0; r < 16; ++r) pmax = fmaxf(pmax, p1[r]);
  { auto rr = __builtin_amdgcn_permlane32_swap(__float_as_uint(pmax), __float_as_uint(pmax), false, false);
    pmax = fmaxf(__uint_as_float(rr[0]), __uint_as_float(rr[1])); }
  if (__builtin_expect(__all(pmax - m_reg <= THR / SCALE), 1)) { mn = m_reg; alpha = 1.f; }
  else { mn = fmaxf(m_reg, pmax); alpha = __builtin_amdgcn_exp2f((m_reg - mn) * C); m_reg = mn; }
  float mnC = -mn * C;
  for (int r = 0; r < 16; ++r) p0[r] = fmaf(p0[r], C, mnC);
  for (int r = 0; r < 16; ++r) p1[r] = fmaf(p1[r], C, mnC);
  for (int r = 0; r < 16; ++r) p0[r] = __builtin_amdgcn_exp2f(p0[r]);
}
__device__ __forceinline__ void finishSM(f32x16& p0, f32x16& p1, float alpha, float& l_reg, bf16x8& pa0, bf16x8& pa1,
                                         bf16x8& pa2, bf16x8& pa3) {
  for (int r = 0; r < 16; ++r) p1[r] = __builtin_amdgcn_exp2f(p1[r]);
  float ps = 0;
  for (int r = 0; r < 16; ++r) ps += p0[r];
  for (int r = 0; r < 16; ++r) ps += p1[r];
  { auto rr = __builtin_amdgcn_permlane32_swap(__float_as_uint(ps), __float_as_uint(ps), false, false);
    ps = __uint_as_float(rr[0]) + __uint_as_float(rr[1]); }
  l_reg = l_reg * alpha + ps;
#define PK4(P, BASE, OUT) do { unsigned a0 = cvtpk(P[BASE + 0], P[BASE + 1]), a1 = cvtpk(P[BASE + 2], P[BASE + 3]);   \
    unsigned b0 = cvtpk(P[BASE + 4], P[BASE + 5]), b1 = cvtpk(P[BASE + 6], P[BASE + 7]);                              \
    auto r0 = __builtin_amdgcn_permlane32_swap(a0, b0, false, false); auto r1 = __builtin_amdgcn_permlane32_swap(a1, b1, false, false); \
    u32x4 w = {r0[0], r1[0], r0[1], r1[1]}; OUT = *reinterpret_cast<bf16x8*>(&w); } while (0)
  PK4(p0, 0, pa0); PK4(p0, 8, pa1); PK4(p1, 0, pa2); PK4(p1, 8, pa3);
#undef PK4
}
__device__ __forceinline__ void qkt(f32x16& p0, f32x16& p1, const char* Ks, const bf16x8* qr, int r32, int hi) {
  p0 = f32x16{}; p1 = f32x16{};
  for (int d0 = 0; d0 < 8; ++d0) { int cb = (d0 * 16 + hi * 8) * 2;
    bf16x8 b0 = *reinterpret_cast<const bf16x8*>(Ks + KSWZ(r32, cb));
    bf16x8 b1 = *reinterpret_cast<const bf16x8*>(Ks + KSWZ(32 + r32, cb));
    p0 = __builtin_amdgcn_mfma_f32_32x32x16_bf16(b0, qr[d0], p0, 0, 0, 0);
    p1 = __builtin_amdgcn_mfma_f32_32x32x16_bf16(b1, qr[d0], p1, 0, 0, 0); }
}
template <int D0>
__device__ __forceinline__ void pv_one(f32x16& od, int vb, bf16x8 pa0, bf16x8 pa1, bf16x8 pa2, bf16x8 pa3) {
  const s16x4 l0 = tr_read<v_rd_off(D0, 0, 0)>(vb), h0 = tr_read<v_rd_off(D0, 0, 1)>(vb), l1 = tr_read<v_rd_off(D0, 1, 0)>(vb), h1 = tr_read<v_rd_off(D0, 1, 1)>(vb);
  const s16x4 l2 = tr_read<v_rd_off(D0, 2, 0)>(vb), h2 = tr_read<v_rd_off(D0, 2, 1)>(vb), l3 = tr_read<v_rd_off(D0, 3, 0)>(vb), h3 = tr_read<v_rd_off(D0, 3, 1)>(vb);
  asm volatile("s_waitcnt lgkmcnt(0)" ::: "memory"); SBAR();
#define PK(L, H) (bf16x8){L[0], L[1], L[2], L[3], H[0], H[1], H[2], H[3]}
  od = __builtin_amdgcn_mfma_f32_32x32x16_bf16(pa0, PK(l0, h0), od, 0, 0, 0);
  od = __builtin_amdgcn_mfma_f32_32x32x16_bf16(pa1, PK(l1, h1), od, 0, 0, 0);
  od = __builtin_amdgcn_mfma_f32_32x32x16_bf16(pa2, PK(l2, h2), od, 0, 0, 0);
  od = __builtin_amdgcn_mfma_f32_32x32x16_bf16(pa3, PK(l3, h3), od, 0, 0, 0);
#undef PK
}
__device__ __forceinline__ void pv_d0(f32x16* o, int vb, bf16x8 pa0, bf16x8 pa1, bf16x8 pa2, bf16x8 pa3) {
  pv_one<0>(o[0], vb, pa0, pa1, pa2, pa3); pv_one<1>(o[1], vb, pa0, pa1, pa2, pa3);
  pv_one<2>(o[2], vb, pa0, pa1, pa2, pa3); pv_one<3>(o[3], vb, pa0, pa1, pa2, pa3);
}

__global__ __launch_bounds__(64 * NW, 1) void k_attn(const u16* __restrict__ Q, const u16* __restrict__ Kc,
                                                     const u16* __restrict__ Vc, u16* __restrict__ O) {
  extern __shared__ __attribute__((aligned(16))) char lds[];
  const int h = blockIdx.y;
  const long i0 = (long)blockIdx.x * (QBLK * NW);
  const u16* Qb = Q + i0 * LDQ + h * HD;
  const u16* Kh = Kc + (long)h * SK * HD;
  const u16* Vh = Vc + (long)h * SK * HD;
  u16* Ob = O + i0 * LDO + h * HD;
  const int tid = threadIdx.x, wid = tid >> 6, lane = tid & 63, r32 = lane & 31, hi = lane >> 5;
  char* V_lds = lds; char* K_lds = lds + 2 * SHM_V;
  float* ws = (float*)(lds + 2 * SHM_V + 2 * SHM_K) + wid * 64; float* li_l = ws; float* al_l = ws + 32;
  float m_reg = -1e30f, l_reg = 0; f32x16 o[4] = {}; bf16x8 qr[8];
  const u16* Qw = Qb + (long)(wid * QBLK + r32) * LDQ + hi * 8;
#pragma unroll
  for (int d0 = 0; d0 < 8; ++d0) qr[d0] = *reinterpret_cast<const bf16x8*>(Qw + d0 * 16);
  const int sr = tid >> 4, sc = (tid & 15) * 8, vst0 = v_st(sr, sc), vst1 = v_st(32 + sr, sc);
  const int vb0 = (int)(uintptr_t)V_lds + v_rd_base(lane);
  struct { bf16x8 vs0, vs1, ks0, ks1; } sr_[2];
#define LD8(p) (*reinterpret_cast<const bf16x8*>(p))
#define SLOAD(i, k0) do { sr_[i].vs0 = LD8(&Vh[(long)((k0) + sr) * LDK + sc]); sr_[i].vs1 = LD8(&Vh[(long)((k0) + 32 + sr) * LDK + sc]); \
    sr_[i].ks0 = LD8(&Kh[(long)((k0) + sr) * LDK + sc]); sr_[i].ks1 = LD8(&Kh[(long)((k0) + 32 + sr) * LDK + sc]); } while (0)
#define SWRITE(b, i) do { *(bf16x8*)(V_lds + (b) * SHM_V + vst0) = sr_[i].vs0;          \
    *(bf16x8*)(V_lds + (b) * SHM_V + vst1) = sr_[i].vs1; int kc = sc * 2;               \
    *(bf16x8*)(K_lds + (b) * SHM_K + KSWZ(sr, kc)) = sr_[i].ks0;                       \
    *(bf16x8*)(K_lds + (b) * SHM_K + KSWZ(32 + sr, kc)) = sr_[i].ks1; } while (0)
#define SWAIT() asm volatile("s_waitcnt vmcnt(4)" ::: "memory")
#define RESC(a) do { if (__any((a) < 1.f)) { if (hi == 0) al_l[r32] = (a); asm volatile("s_waitcnt lgkmcnt(0)" ::: "memory"); \
    for (int d = 0; d < 4; ++d) for (int r = 0; r < 16; ++r) o[d][r] *= al_l[crow(r, hi)]; } } while (0)
  f32x16 pA0, pA1, pB0, pB1; float mnA, mnB, alA, alB; bf16x8 pa0, pa1, pa2, pa3; const int NT = SK / KVBLK;
  constexpr int SE = 0, SO = 1;
  SLOAD(SE, 0); asm volatile("s_waitcnt vmcnt(0)" ::: "memory"); SWRITE(0, SE); __syncthreads();
  qkt(pA0, pA1, K_lds, qr, r32, hi); partialSM(pA0, pA1, m_reg, mnA, alA);
  SLOAD(SO, KVBLK); SLOAD(SE, 2 * KVBLK);
  SWAIT(); SWRITE(1, SO); __syncthreads();
  for (int j = 1; j + 1 < NT; j += 2) {
    SBAR(); qkt(pB0, pB1, K_lds + SHM_K, qr, r32, hi);
    finishSM(pA0, pA1, alA, l_reg, pa0, pa1, pa2, pa3); SBAR();
    SLOAD(SO, (j + 2) * KVBLK); SBAR();
    pv_d0(o, vb0, pa0, pa1, pa2, pa3); partialSM(pB0, pB1, m_reg, mnB, alB);
    __syncthreads(); SWAIT(); SWRITE(0, SE);
    RESC(alB); __syncthreads();
    SBAR(); qkt(pA0, pA1, K_lds, qr, r32, hi);
    finishSM(pB0, pB1, alB, l_reg, pa0, pa1, pa2, pa3); SBAR();
    if (j + 3 < NT) SLOAD(SE, (j + 3) * KVBLK); SBAR();
    pv_d0(o, vb0 + (int)SHM_V, pa0, pa1, pa2, pa3); partialSM(pA0, pA1, m_reg, mnA, alA);
    __syncthreads(); SWAIT(); SWRITE(1, SO);
    RESC(alA); __syncthreads();
  }
  SBAR(); qkt(pB0, pB1, K_lds + SHM_K, qr, r32, hi);
  finishSM(pA0, pA1, alA, l_reg, pa0, pa1, pa2, pa3); SBAR();
  pv_d0(o, vb0, pa0, pa1, pa2, pa3); partialSM(pB0, pB1, m_reg, mnB, alB);
  __syncthreads(); RESC(alB);
  finishSM(pB0, pB1, alB, l_reg, pa0, pa1, pa2, pa3); SBAR();
  pv_d0(o, vb0 + (int)SHM_V, pa0, pa1, pa2, pa3);
  if (hi == 0) li_l[r32] = l_reg;
  asm volatile("s_waitcnt lgkmcnt(0)" ::: "memory");
  float rli[16];
#pragma unroll
  for (int r = 0; r < 16; ++r) rli[r] = __builtin_amdgcn_rcpf(li_l[crow(r, hi)]);
  u16* Ow = Ob + (long)(wid * QBLK) * LDO;
#pragma unroll
  for (int r = 0; r < 16; ++r) { int orow = crow(r, hi);
    for (int d0 = 0; d0 < 4; ++d0) Ow[(long)orow * LDO + d0 * 32 + r32] = f2bf(o[d0][r] * rli[r]); }
#undef SLOAD
#undef SWRITE
#undef SWAIT
#undef RESC
#undef LD8
}

extern "C" void kernel_launch(void* const* d_in, const int* in_sizes, int n_in, void* d_out, int out_size, void* d_ws,
                              size_t ws_size, hipStream_t stream) {
  const u16* x = (const u16*)d_in[0];
  const u16* kc = (const u16*)d_in[1];
  const u16* vc = (const u16*)d_in[2];
  const u16* wq = (const u16*)d_in[3];
  const u16* wo = (const u16*)d_in[4];
  const u16* w_in = (const u16*)d_in[6];
  const u16* w_sp = (const u16*)d_in[7];
  const u16* b_sp = (const u16*)d_in[8];
  const u16* w_out = (const u16*)d_in[9];
  const u16* cvec = (const u16*)d_in[10];
  const u16* w_mod = (const u16*)d_in[11];
  u16* out = (u16*)d_out;

  char* ws = (char*)d_ws;
  size_t off = 0;
  auto alloc = [&](size_t bytes) { char* p = ws + off; off += (bytes + 255) & ~(size_t)255; return p; };
  float* mod = (float*)alloc(6 * DM * 4);
  u16* h1 = (u16*)alloc((size_t)SEQ * DM * 2);
  u16* q = (u16*)alloc((size_t)SEQ * NH * HD * 2);
  u16* o = (u16*)alloc((size_t)SEQ * NH * HD * 2);
  float* part = (float*)alloc((size_t)SPLITA * SEQ * DM * 4);
  u16* x1 = (u16*)alloc((size_t)SEQ * DM * 2);
  u16* h2 = (u16*)alloc((size_t)SEQ * DM * 2);
  u16* z1 = (u16*)alloc((size_t)SEQ * FH * 2);
  u16* z2 = (u16*)alloc((size_t)SEQ * FH * 2);
  u16* z2n = (u16*)alloc((size_t)SEQ * FH * 2);
  u16* u = (u16*)alloc((size_t)SEQ * FH * 2);

  (void)hipFuncSetAttribute((const void*)k_attn, hipFuncAttributeMaxDynamicSharedMemorySize, (int)SHM_ATTN);

  k_mod<<<dim3(6 * DM / 64), dim3(256), 0, stream>>>(cvec, w_mod, mod);
  k_ln_mod<<<dim3(SEQ / 4), dim3(256), 0, stream>>>(x, mod, h1);
  {
    GemmArgs g{h1, wq, q, nullptr, nullptr, nullptr, nullptr, DM, NH * HD, NH * HD, DM};
    k_gemm<128, 2, 2, EPI_BF16><<<dim3(NH * HD / 128, SEQ / 128, 1), dim3(256), 0, stream>>>(g);
  }
  k_attn<<<dim3(SEQ / 256, NH), dim3(512), SHM_ATTN, stream>>>(q, kc, vc, o);
  {
    GemmArgs g{o, wo, part, nullptr, nullptr, nullptr, nullptr, NH * HD, DM, DM, NH * HD / SPLITA};
    k_gemm<128, 2, 2, EPI_F32PART><<<dim3(DM / 128, SEQ / 128, SPLITA), dim3(256), 0, stream>>>(g);
  }
  k_reduce_ln<<<dim3(SEQ / 4), dim3(256), 0, stream>>>(part, x, mod, x1, h2);
  {
    GemmArgs g{h2, w_in, z1, z2, nullptr, nullptr, nullptr, DM, FF, FH, DM};
    k_gemm<128, 2, 2, EPI_GELU><<<dim3(FF / 128, SEQ / 128, 1), dim3(256), 0, stream>>>(g);
  }
  k_ln_z2<<<dim3(SEQ / 4), dim3(256), 0, stream>>>(z2, z2n);
  {
    GemmArgs g{w_sp, z2n, u, nullptr, b_sp, z1, nullptr, SEQ, FH, FH, SEQ};
    k_gemm<64, 1, 4, EPI_GATE><<<dim3(FH / 128, SEQ / 64, 1), dim3(256), 0, stream>>>(g);
  }
  {
    GemmArgs g{u, w_out, out, nullptr, x1, nullptr, mod, FH, DM, DM, FH};
    k_gemm<32, 1, 4, EPI_FINAL><<<dim3(DM / 128, SEQ / 32, 1), dim3(256), 0, stream>>>(g);
  }
}
```

```cpp
#include <hip/hip_runtime.h>
#include <hip/hip_bf16.h>
#include <stdint.h>


typedef unsigned short u16;
typedef short bf16x8 __attribute__((ext_vector_type(8)));
typedef short s16x4 __attribute__((ext_vector_type(4)));
typedef float f32x16 __attribute__((ext_vector_type(16)));
typedef float f32x4 __attribute__((ext_vector_type(4)));
typedef unsigned u32x4 __attribute__((ext_vector_type(4)));

constexpr int SEQ = 2048, DM = 512, NH = 32, HD = 128, SK = 2048, FH = 896, FF = 1792;
constexpr float LN_EPS = 1e-5f;

__device__ __forceinline__ u16 f2bf(float f) {
  unsigned u = __float_as_uint(f);
  u += 0x7fffu + ((u >> 16) & 1u);
  return (u16)(u >> 16);
}
__device__ __forceinline__ float bf2f(u16 b) { return __uint_as_float(((unsigned)b) << 16); }
__device__ __forceinline__ float rbf(float f) { return bf2f(f2bf(f)); }
__device__ __forceinline__ int crow(int r, int hi) { return (r & 3) + 8 * (r >> 2) + 4 * hi; }
__device__ __forceinline__ unsigned cvtpk(float lo, float hi) {
  unsigned r;
  asm volatile("v_cvt_pk_bf16_f32 %0, %1, %2" : "=v"(r) : "v"(lo), "v"(hi));
  return r;
}
#define SBAR() __builtin_amdgcn_sched_barrier(0)

__global__ __launch_bounds__(256) void k_mod(const u16* __restrict__ c, const u16* __restrict__ wmod,
                                             float* __restrict__ mod) {
  __shared__ float red[32][65];
  __shared__ float sc[DM];
  const int tid = threadIdx.x;
  for (int i = tid; i < DM; i += 256) {
    float x = bf2f(c[i]);
    sc[i] = rbf(x / (1.f + __expf(-x)));
  }
  __syncthreads();
  const int cc = tid & 7, kg = tid >> 3;
  const int j0 = blockIdx.x * 64 + cc * 8;
  bf16x8 w[16];
#pragma unroll
  for (int i = 0; i < 16; ++i) w[i] = *reinterpret_cast<const bf16x8*>(wmod + (size_t)(kg * 16 + i) * (6 * DM) + j0);
  float acc[8];
#pragma unroll
  for (int e = 0; e < 8; ++e) acc[e] = 0.f;
#pragma unroll
  for (int i = 0; i < 16; ++i) {
    const float sv = sc[kg * 16 + i];
#pragma unroll
    for (int e = 0; e < 8; ++e) acc[e] += sv * bf2f((u16)w[i][e]);
  }
#pragma unroll
  for (int e = 0; e < 8; ++e) red[kg][cc * 8 + e] = acc[e];
  __syncthreads();
  if (tid < 64) {
    float t = 0.f;
#pragma unroll
    for (int g = 0; g < 32; ++g) t += red[g][tid];
    mod[blockIdx.x * 64 + tid] = rbf(t);
  }
}

__device__ __forceinline__ float wave_sum(float v) {
#pragma unroll
  for (int o = 32; o >= 1; o >>= 1) v += __shfl_xor(v, o, 64);
  return v;
}

__global__ __launch_bounds__(256) void k_ln_mod(const u16* __restrict__ x, const float* __restrict__ mod,
                                                u16* __restrict__ h1) {
  const int wid = threadIdx.x >> 6, lane = threadIdx.x & 63;
  const int row = blockIdx.x * 4 + wid;
  const bf16x8 raw = *reinterpret_cast<const bf16x8*>(x + (size_t)row * DM + lane * 8);
  float v[8];
  float s = 0.f;
#pragma unroll
  for (int i = 0; i < 8; ++i) { v[i] = bf2f((u16)raw[i]); s += v[i]; }
  s = wave_sum(s);
  const float mean = s * (1.f / DM);
  float q = 0.f;
#pragma unroll
  for (int i = 0; i < 8; ++i) { float d = v[i] - mean; q += d * d; }
  q = wave_sum(q);
  const float mb = rbf(mean), vb = rbf(q * (1.f / DM));
  const float den = rbf(sqrtf(rbf(vb + LN_EPS)));
  bf16x8 o;
#pragma unroll
  for (int i = 0; i < 8; ++i) {
    const int cidx = lane * 8 + i;
    const float sa = mod[cidx], ca = mod[DM + cidx];
    float n = rbf(rbf(v[i] - mb) / den);
    float h = rbf(rbf(n * rbf(1.f + ca)) + sa);
    o[i] = (short)f2bf(h);
  }
  *reinterpret_cast<bf16x8*>(h1 + (size_t)row * DM + lane * 8) = o;
}

__device__ __forceinline__ int a_off(int row, int colB) { return row * 128 + (colB ^ (((row >> 1) & 7) << 4)); }
__device__ __forceinline__ int v_st(int k, int c) {
  const int kk = (k & ~0xC) | ((k & 4) << 1) | ((k & 8) >> 1);
  return ((kk >> 3) * 4 + (c >> 5)) * 512 + ((kk & 7) * 32 + (c & 31)) * 2;
}
__device__ __forceinline__ int v_rd_base(int lane) {
  return ((lane & 3) << 3) | (((lane >> 2) & 3) << 6) | (((lane >> 4) & 1) << 5) | (((lane >> 5) & 1) << 8);
}
constexpr int v_rd_off(int d0, int ks, int half) { return d0 * 512 + ks * 4096 + half * 2048; }
template <int OFF>
__device__ __forceinline__ s16x4 tr_read(int vb) {
  s16x4 r;
  asm volatile("ds_read_b64_tr_b16 %0, %1 offset:%2" : "=&v"(r) : "v"(vb), "i"(OFF) : "memory");
  return r;
}

enum { EPI_BF16 = 0, EPI_F32PART = 1, EPI_GELU = 2, EPI_GATE = 3, EPI_FINAL = 4 };

struct GemmArgs {
  const u16* A; const u16* B;
  void* C; void* C2;
  const u16* aux0; const u16* aux1; const float* mod;
  int lda, ldb, ldc, K;
};

template <int OFFK>
__device__ __forceinline__ void b_frag(int vb, int j, s16x4& lo, s16x4& hi) {
  if (j == 0) { lo = tr_read<v_rd_off(0, OFFK, 0)>(vb); hi = tr_read<v_rd_off(0, OFFK, 1)>(vb); }
  else if (j == 1) { lo = tr_read<v_rd_off(1, OFFK, 0)>(vb); hi = tr_read<v_rd_off(1, OFFK, 1)>(vb); }
  else if (j == 2) { lo = tr_read<v_rd_off(2, OFFK, 0)>(vb); hi = tr_read<v_rd_off(2, OFFK, 1)>(vb); }
  else { lo = tr_read<v_rd_off(3, OFFK, 0)>(vb); hi = tr_read<v_rd_off(3, OFFK, 1)>(vb); }
}
template <int N> __device__ __forceinline__ void wait_vm() {
  if constexpr (N == 0) asm volatile("s_waitcnt vmcnt(0)" ::: "memory");
  else if constexpr (N == 1) asm volatile("s_waitcnt vmcnt(1)" ::: "memory");
  else if constexpr (N == 2) asm volatile("s_waitcnt vmcnt(2)" ::: "memory");
  else if constexpr (N == 3) asm volatile("s_waitcnt vmcnt(3)" ::: "memory");
  else if constexpr (N == 4) asm volatile("s_waitcnt vmcnt(4)" ::: "memory");
  else if constexpr (N == 5) asm volatile("s_waitcnt vmcnt(5)" ::: "memory");
  else if constexpr (N == 6) asm volatile("s_waitcnt vmcnt(6)" ::: "memory");
  else if constexpr (N == 8) asm volatile("s_waitcnt vmcnt(8)" ::: "memory");
  else if constexpr (N == 9) asm volatile("s_waitcnt vmcnt(9)" ::: "memory");
  else if constexpr (N == 10) asm volatile("s_waitcnt vmcnt(10)" ::: "memory");
  else if constexpr (N == 12) asm volatile("s_waitcnt vmcnt(12)" ::: "memory");
  else if constexpr (N == 15) asm volatile("s_waitcnt vmcnt(15)" ::: "memory");
  else static_assert(N < 0, "unsupported vmcnt");
}

template <int BM, int WM, int WN, int NS, int EPI>
__global__ __launch_bounds__(64 * WM * WN) void k_gemm(GemmArgs g) {
  constexpr int BN = 128, BK = 64, NWV = WM * WN;
  constexpr int TM = BM / WM / 32, TN = BN / WN / 32;
  constexpr int STG = BM * 128 + 16384;
  constexpr int LPW = STG / 1024 / NWV;
  static_assert(LPW * 1024 * NWV == STG, "stage must split evenly over waves");
  extern __shared__ __attribute__((aligned(16))) char lds[];
  const int tid = threadIdx.x, wid = tid >> 6, lane = tid & 63, r32 = lane & 31, hi = lane >> 5;
  const int wm = wid / WN, wn = wid % WN;
  const int bm = blockIdx.y * BM, bn = blockIdx.x * BN;
  const int kbase = blockIdx.z * g.K;
  const u16* Ab = g.A + (size_t)bm * g.lda + kbase;
  const u16* Bb = g.B + (size_t)kbase * g.ldb + bn;
  const u16* src[LPW];
#pragma unroll
  for (int i = 0; i < LPW; ++i) {
    const int p = (i * NWV + wid) * 1024 + lane * 16;
    if (p < BM * 128) {
      const int ci = p >> 4, row = ci >> 3, slot = ci & 7, cch = slot ^ ((row >> 1) & 7);
      src[i] = Ab + (size_t)row * g.lda + cch * 8;
    } else {
      const int q = p - BM * 128, st = q >> 9, rin = (q >> 6) & 7, cch = (q >> 4) & 3;
      const int kk = (st >> 2) * 8 + rin, c = (st & 3) * 32 + cch * 8;
      const int k = (kk & ~0xC) | ((kk & 4) << 1) | ((kk & 8) >> 1);
      src[i] = Bb + (size_t)k * g.ldb + c;
    }
  }
  auto stage = [&](int kt) {
    char* dst = lds + (kt % NS) * STG;
#pragma unroll
    for (int i = 0; i < LPW; ++i) {
      const int p = (i * NWV + wid) * 1024;
      const u16* sp = src[i] + ((p < BM * 128) ? (size_t)kt * BK : (size_t)kt * BK * g.ldb);
      __builtin_amdgcn_global_load_lds((const unsigned*)sp, (unsigned*)(dst + p), 16, 0, 0);
    }
  };
  f32x16 acc[TM][TN];
#pragma unroll
  for (int i = 0; i < TM; ++i)
#pragma unroll
    for (int j = 0; j < TN; ++j) acc[i][j] = f32x16{};

  const int nk = g.K / BK;
#pragma unroll
  for (int s = 0; s < NS - 1; ++s)
    if (s < nk) stage(s);
  const int vbB = (int)(uintptr_t)lds + BM * 128 + v_rd_base(lane) + wn * TN * 512;
  for (int kt = 0; kt < nk; ++kt) {
    const int rem = nk - 1 - kt;
    if (rem >= NS - 2) wait_vm<LPW*(NS - 2)>();
    else if (NS > 3 && rem == 1) wait_vm<LPW>();
    else wait_vm<0>();
    asm volatile("s_waitcnt lgkmcnt(0)" ::: "memory");
    __builtin_amdgcn_s_barrier();
    if (kt + NS - 1 < nk) stage(kt + NS - 1);
    const char* Ac = lds + (kt % NS) * STG;
    const int vb = vbB + (kt % NS) * STG;
#pragma unroll
    for (int ks = 0; ks < 4; ++ks) {
      bf16x8 af[TM];
#pragma unroll
      for (int i = 0; i < TM; ++i)
        af[i] = *reinterpret_cast<const bf16x8*>(Ac + a_off(wm * (BM / WM) + i * 32 + r32, (ks * 16 + hi * 8) * 2));
      s16x4 bl[TN], bh[TN];
#pragma unroll
      for (int j = 0; j < TN; ++j) {
        if (ks == 0) b_frag<0>(vb, j, bl[j], bh[j]);
        else if (ks == 1) b_frag<1>(vb, j, bl[j], bh[j]);
        else if (ks == 2) b_frag<2>(vb, j, bl[j], bh[j]);
        else b_frag<3>(vb, j, bl[j], bh[j]);
      }
      asm volatile("s_waitcnt lgkmcnt(0)" ::: "memory");
      SBAR();
#pragma unroll
      for (int j = 0; j < TN; ++j) {
        const bf16x8 bfr = {bl[j][0], bl[j][1], bl[j][2], bl[j][3], bh[j][0], bh[j][1], bh[j][2], bh[j][3]};
#pragma unroll
        for (int i = 0; i < TM; ++i) acc[i][j] = __builtin_amdgcn_mfma_f32_32x32x16_bf16(af[i], bfr, acc[i][j], 0, 0, 0);
      }
    }
  }
#pragma unroll
  for (int i = 0; i < TM; ++i)
#pragma unroll
    for (int j = 0; j < TN; ++j) {
      const int col = bn + wn * (BN / WN) + j * 32 + r32;
#pragma unroll
      for (int r = 0; r < 16; ++r) {
        const int row = bm + wm * (BM / WM) + i * 32 + crow(r, hi);
        const float v = acc[i][j][r];
        if constexpr (EPI == EPI_BF16) {
          ((u16*)g.C)[(size_t)row * g.ldc + col] = f2bf(v);
        } else if constexpr (EPI == EPI_F32PART) {
          ((float*)g.C)[((size_t)blockIdx.z * SEQ + row) * g.ldc + col] = v;
        } else if constexpr (EPI == EPI_GELU) {
          const float xb = rbf(v);
          const float t = 0.7978845608028654f * (xb + 0.044715f * xb * xb * xb);
          const float ge = 0.5f * xb * (1.f + tanhf(t));
          if (col < FH) ((u16*)g.C)[(size_t)row * FH + col] = f2bf(ge);
          else ((u16*)g.C2)[(size_t)row * FH + (col - FH)] = f2bf(ge);
        } else if constexpr (EPI == EPI_GATE) {
          const float gg = rbf(rbf(v) + bf2f(g.aux0[row]));
          const float z1 = bf2f(g.aux1[(size_t)row * FH + col]);
          ((u16*)g.C)[(size_t)row * FH + col] = f2bf(z1 * gg);
        } else {
          const float o = rbf(v);
          const float gb = g.mod[5 * DM + col];
          const float x1 = bf2f(g.aux0[(size_t)row * DM + col]);
          ((u16*)g.C)[(size_t)row * DM + col] = f2bf(x1 + rbf(gb * o));
        }
      }
    }
}
template <int BM, int WM, int WN, int NS, int EPI>
static void launch_gemm(const GemmArgs& g, int M, int N, int splits, hipStream_t stream) {
  constexpr int shm = NS * (BM * 128 + 16384);
  (void)hipFuncSetAttribute((const void*)k_gemm<BM, WM, WN, NS, EPI>, hipFuncAttributeMaxDynamicSharedMemorySize, shm);
  k_gemm<BM, WM, WN, NS, EPI><<<dim3(N / 128, M / BM, splits), dim3(64 * WM * WN), shm, stream>>>(g);
}

constexpr int SPLITA = 4;
__global__ __launch_bounds__(256) void k_reduce_ln(const float* __restrict__ part, const u16* __restrict__ x,
                                                   const float* __restrict__ mod, u16* __restrict__ x1,
                                                   u16* __restrict__ h2) {
  const int wid = threadIdx.x >> 6, lane = threadIdx.x & 63;
  const int row = blockIdx.x * 4 + wid;
  const bf16x8 raw = *reinterpret_cast<const bf16x8*>(x + (size_t)row * DM + lane * 8);
  float a[8];
#pragma unroll
  for (int i = 0; i < 8; ++i) a[i] = 0.f;
#pragma unroll
  for (int s = 0; s < SPLITA; ++s) {
    const f32x4* p = reinterpret_cast<const f32x4*>(part + ((size_t)s * SEQ + row) * DM + lane * 8);
    f32x4 p0 = p[0], p1 = p[1];
#pragma unroll
    for (int i = 0; i < 4; ++i) { a[i] += p0[i]; a[4 + i] += p1[i]; }
  }
  float v[8];
  float s = 0.f;
#pragma unroll
  for (int i = 0; i < 8; ++i) {
    const int cidx = lane * 8 + i;
    const float ga = mod[2 * DM + cidx];
    v[i] = rbf(bf2f((u16)raw[i]) + rbf(ga * rbf(a[i])));
    s += v[i];
  }
  s = wave_sum(s);
  const float mean = s * (1.f / DM);
  float q = 0.f;
#pragma unroll
  for (int i = 0; i < 8; ++i) { float d = v[i] - mean; q += d * d; }
  q = wave_sum(q);
  const float mb = rbf(mean), vb = rbf(q * (1.f / DM));
  const float den = rbf(sqrtf(rbf(vb + LN_EPS)));
  bf16x8 o, ox;
#pragma unroll
  for (int i = 0; i < 8; ++i) {
    const int cidx = lane * 8 + i;
    const float sb = mod[3 * DM + cidx], cb = mod[4 * DM + cidx];
    float n = rbf(rbf(v[i] - mb) / den);
    float h = rbf(rbf(n * rbf(1.f + cb)) + sb);
    o[i] = (short)f2bf(h);
    ox[i] = (short)f2bf(v[i]);
  }
  *reinterpret_cast<bf16x8*>(h2 + (size_t)row * DM + lane * 8) = o;
  *reinterpret_cast<bf16x8*>(x1 + (size_t)row * DM + lane * 8) = ox;
}

__global__ __launch_bounds__(256) void k_ln_z2(const u16* __restrict__ z2, u16* __restrict__ z2n) {
  const int wid = threadIdx.x >> 6, lane = threadIdx.x & 63;
  const int row = blockIdx.x * 4 + wid;
  const unsigned* p = reinterpret_cast<const unsigned*>(z2 + (size_t)row * FH);
  float v[14];
  float s = 0.f;
#pragma unroll
  for (int i = 0; i < 7; ++i) {
    unsigned w = p[i * 64 + lane];
    v[2 * i] = bf2f((u16)(w & 0xffff));
    v[2 * i + 1] = bf2f((u16)(w >> 16));
    s += v[2 * i] + v[2 * i + 1];
  }
  s = wave_sum(s);
  const float mean = s * (1.f / FH);
  float q = 0.f;
#pragma unroll
  for (int i = 0; i < 14; ++i) { float d = v[i] - mean; q += d * d; }
  q = wave_sum(q);
  const float mb = rbf(mean), vb = rbf(q * (1.f / FH));
  const float den = rbf(sqrtf(rbf(vb + LN_EPS)));
  unsigned* o = reinterpret_cast<unsigned*>(z2n + (size_t)row * FH);
#pragma unroll
  for (int i = 0; i < 7; ++i) {
    const u16 a = f2bf(rbf(v[2 * i] - mb) / den), b = f2bf(rbf(v[2 * i + 1] - mb) / den);
    o[i * 64 + lane] = (unsigned)a | ((unsigned)b << 16);
  }
}

constexpr int NW = 8, QBLK = 32, KVBLK = 64;
constexpr float SCALE = 0.088388347648318440f;
constexpr float THR = 8.f;
constexpr int LDQ = NH * HD, LDK = HD, LDO = NH * HD;
constexpr size_t SHM_V = KVBLK * HD * 2, SHM_K = KVBLK * HD * 2, SHM_ATTN = 2 * SHM_V + 2 * SHM_K + NW * 64 * 4;
#define KSWZ(row, colB) ((row) * 256 + ((colB) ^ (((row) & 7) << 4)))

__device__ __forceinline__ void partialSM(f32x16& p0, f32x16& p1, float& m_reg, float& mn, float& alpha) {
  constexpr float C = SCALE * 1.4426950408889634f;
  float pmax = p0[0];
  for (int r = 1; r < 16; ++r) pmax = fmaxf(pmax, p0[r]);
  for (int r = 0; r < 16; ++r) pmax = fmaxf(pmax, p1[r]);
  { auto rr = __builtin_amdgcn_permlane32_swap(__float_as_uint(pmax), __float_as_uint(pmax), false, false);
    pmax = fmaxf(__uint_as_float(rr[0]), __uint_as_float(rr[1])); }
  if (__builtin_expect(__all(pmax - m_reg <= THR / SCALE), 1)) { mn = m_reg; alpha = 1.f; }
  else { mn = fmaxf(m_reg, pmax); alpha = __builtin_amdgcn_exp2f((m_reg - mn) * C); m_reg = mn; }
  float mnC = -mn * C;
  for (int r = 0; r < 16; ++r) p0[r] = fmaf(p0[r], C, mnC);
  for (int r = 0; r < 16; ++r) p1[r] = fmaf(p1[r], C, mnC);
  for (int r = 0; r < 16; ++r) p0[r] = __builtin_amdgcn_exp2f(p0[r]);
}
__device__ __forceinline__ void finishSM(f32x16& p0, f32x16& p1, float alpha, float& l_reg, bf16x8& pa0, bf16x8& pa1,
                                         bf16x8& pa2, bf16x8& pa3) {
  for (int r = 0; r < 16; ++r) p1[r] = __builtin_amdgcn_exp2f(p1[r]);
  float ps = 0;
  for (int r = 0; r < 16; ++r) ps += p0[r];
  for (int r = 0; r < 16; ++r) ps += p1[r];
  { auto rr = __builtin_amdgcn_permlane32_swap(__float_as_uint(ps), __float_as_uint(ps), false, false);
    ps = __uint_as_float(rr[0]) + __uint_as_float(rr[1]); }
  l_reg = l_reg * alpha + ps;
#define PK4(P, BASE, OUT) do { unsigned a0 = cvtpk(P[BASE + 0], P[BASE + 1]), a1 = cvtpk(P[BASE + 2], P[BASE + 3]);   \
    unsigned b0 = cvtpk(P[BASE + 4], P[BASE + 5]), b1 = cvtpk(P[BASE + 6], P[BASE + 7]);                              \
    auto r0 = __builtin_amdgcn_permlane32_swap(a0, b0, false, false); auto r1 = __builtin_amdgcn_permlane32_swap(a1, b1, false, false); \
    u32x4 w = {r0[0], r1[0], r0[1], r1[1]}; OUT = *reinterpret_cast<bf16x8*>(&w); } while (0)
  PK4(p0, 0, pa0); PK4(p0, 8, pa1); PK4(p1, 0, pa2); PK4(p1, 8, pa3);
#undef PK4
}
__device__ __forceinline__ void qkt(f32x16& p0, f32x16& p1, const char* Ks, const bf16x8* qr, int r32, int hi) {
  p0 = f32x16{}; p1 = f32x16{};
  for (int d0 = 0; d0 < 8; ++d0) { int cb = (d0 * 16 + hi * 8) * 2;
    bf16x8 b0 = *reinterpret_cast<const bf16x8*>(Ks + KSWZ(r32, cb));
    bf16x8 b1 = *reinterpret_cast<const bf16x8*>(Ks + KSWZ(32 + r32, cb));
    p0 = __builtin_amdgcn_mfma_f32_32x32x16_bf16(b0, qr[d0], p0, 0, 0, 0);
    p1 = __builtin_amdgcn_mfma_f32_32x32x16_bf16(b1, qr[d0], p1, 0, 0, 0); }
}
template <int D0>
__device__ __forceinline__ void pv_one(f32x16& od, int vb, bf16x8 pa0, bf16x8 pa1, bf16x8 pa2, bf16x8 pa3) {
  const s16x4 l0 = tr_read<v_rd_off(D0, 0, 0)>(vb), h0 = tr_read<v_rd_off(D0, 0, 1)>(vb), l1 = tr_read<v_rd_off(D0, 1, 0)>(vb), h1 = tr_read<v_rd_off(D0, 1, 1)>(vb);
  const s16x4 l2 = tr_read<v_rd_off(D0, 2, 0)>(vb), h2 = tr_read<v_rd_off(D0, 2, 1)>(vb), l3 = tr_read<v_rd_off(D0, 3, 0)>(vb), h3 = tr_read<v_rd_off(D0, 3, 1)>(vb);
  asm volatile("s_waitcnt lgkmcnt(0)" ::: "memory"); SBAR();
#define PK(L, H) (bf16x8){L[0], L[1], L[2], L[3], H[0], H[1], H[2], H[3]}
  od = __builtin_amdgcn_mfma_f32_32x32x16_bf16(pa0, PK(l0, h0), od, 0, 0, 0);
  od = __builtin_amdgcn_mfma_f32_32x32x16_bf16(pa1, PK(l1, h1), od, 0, 0, 0);
  od = __builtin_amdgcn_mfma_f32_32x32x16_bf16(pa2, PK(l2, h2), od, 0, 0, 0);
  od = __builtin_amdgcn_mfma_f32_32x32x16_bf16(pa3, PK(l3, h3), od, 0, 0, 0);
#undef PK
}
__device__ __forceinline__ void pv_d0(f32x16* o, int vb, bf16x8 pa0, bf16x8 pa1, bf16x8 pa2, bf16x8 pa3) {
  pv_one<0>(o[0], vb, pa0, pa1, pa2, pa3); pv_one<1>(o[1], vb, pa0, pa1, pa2, pa3);
  pv_one<2>(o[2], vb, pa0, pa1, pa2, pa3); pv_one<3>(o[3], vb, pa0, pa1, pa2, pa3);
}

__global__ __launch_bounds__(64 * NW, 1) void k_attn(const u16* __restrict__ Q, const u16* __restrict__ Kc,
                                                     const u16* __restrict__ Vc, u16* __restrict__ O) {
  extern __shared__ __attribute__((aligned(16))) char lds[];
  const int bid = blockIdx.x;
  const int h = (bid & 7) + 8 * (bid >> 6);
  const long i0 = (long)((bid >> 3) & 7) * (QBLK * NW);
  const u16* Qb = Q + i0 * LDQ + h * HD;
  const u16* Kh = Kc + (long)h * SK * HD;
  const u16* Vh = Vc + (long)h * SK * HD;
  u16* Ob = O + i0 * LDO + h * HD;
  const int tid = threadIdx.x, wid = tid >> 6, lane = tid & 63, r32 = lane & 31, hi = lane >> 5;
  char* V_lds = lds; char* K_lds = lds + 2 * SHM_V;
  float* ws = (float*)(lds + 2 * SHM_V + 2 * SHM_K) + wid * 64; float* li_l = ws; float* al_l = ws + 32;
  float m_reg = -1e30f, l_reg = 0; f32x16 o[4] = {}; bf16x8 qr[8];
  const u16* Qw = Qb + (long)(wid * QBLK + r32) * LDQ + hi * 8;
#pragma unroll
  for (int d0 = 0; d0 < 8; ++d0) qr[d0] = *reinterpret_cast<const bf16x8*>(Qw + d0 * 16);
  const int sr = tid >> 4, sc = (tid & 15) * 8, vst0 = v_st(sr, sc), vst1 = v_st(32 + sr, sc);
  const int vb0 = (int)(uintptr_t)V_lds + v_rd_base(lane);
  struct { bf16x8 vs0, vs1, ks0, ks1; } sr_[2];
#define LD8(p) (*reinterpret_cast<const bf16x8*>(p))
#define SLOAD(i, k0) do { sr_[i].vs0 = LD8(&Vh[(long)((k0) + sr) * LDK + sc]); sr_[i].vs1 = LD8(&Vh[(long)((k0) + 32 + sr) * LDK + sc]); \
    sr_[i].ks0 = LD8(&Kh[(long)((k0) + sr) * LDK + sc]); sr_[i].ks1 = LD8(&Kh[(long)((k0) + 32 + sr) * LDK + sc]); } while (0)
#define SWRITE(b, i) do { *(bf16x8*)(V_lds + (b) * SHM_V + vst0) = sr_[i].vs0;          \
    *(bf16x8*)(V_lds + (b) * SHM_V + vst1) = sr_[i].vs1; int kc = sc * 2;               \
    *(bf16x8*)(K_lds + (b) * SHM_K + KSWZ(sr, kc)) = sr_[i].ks0;                       \
    *(bf16x8*)(K_lds + (b) * SHM_K + KSWZ(32 + sr, kc)) = sr_[i].ks1; } while (0)
#define SWAIT() asm volatile("s_waitcnt vmcnt(4)" ::: "memory")
#define RESC(a) do { if (__any((a) < 1.f)) { if (hi == 0) al_l[r32] = (a); asm volatile("s_waitcnt lgkmcnt(0)" ::: "memory"); \
    for (int d = 0; d < 4; ++d) for (int r = 0; r < 16; ++r) o[d][r] *= al_l[crow(r, hi)]; } } while (0)
  f32x16 pA0, pA1, pB0, pB1; float mnA, mnB, alA, alB; bf16x8 pa0, pa1, pa2, pa3; const int NT = SK / KVBLK;
  constexpr int SE = 0, SO = 1;
  SLOAD(SE, 0); asm volatile("s_waitcnt vmcnt(0)" ::: "memory"); SWRITE(0, SE); __syncthreads();
  qkt(pA0, pA1, K_lds, qr, r32, hi); partialSM(pA0, pA1, m_reg, mnA, alA);
  SLOAD(SO, KVBLK); SLOAD(SE, 2 * KVBLK);
  SWAIT(); SWRITE(1, SO); __syncthreads();
  for (int j = 1; j + 1 < NT; j += 2) {
    SBAR(); qkt(pB0, pB1, K_lds + SHM_K, qr, r32, hi);
    finishSM(pA0, pA1, alA, l_reg, pa0, pa1, pa2, pa3); SBAR();
    SLOAD(SO, (j + 2) * KVBLK); SBAR();
    pv_d0(o, vb0, pa0, pa1, pa2, pa3); partialSM(pB0, pB1, m_reg, mnB, alB);
    __syncthreads(); SWAIT(); SWRITE(0, SE);
    RESC(alB); __syncthreads();
    SBAR(); qkt(pA0, pA1, K_lds, qr, r32, hi);
    finishSM(pB0, pB1, alB, l_reg, pa0, pa1, pa2, pa3); SBAR();
    if (j + 3 < NT) SLOAD(SE, (j + 3) * KVBLK); SBAR();
    pv_d0(o, vb0 + (int)SHM_V, pa0, pa1, pa2, pa3); partialSM(pA0, pA1, m_reg, mnA, alA);
    __syncthreads(); SWAIT(); SWRITE(1, SO);
    RESC(alA); __syncthreads();
  }
  SBAR(); qkt(pB0, pB1, K_lds + SHM_K, qr, r32, hi);
  finishSM(pA0, pA1, alA, l_reg, pa0, pa1, pa2, pa3); SBAR();
  pv_d0(o, vb0, pa0, pa1, pa2, pa3); partialSM(pB0, pB1, m_reg, mnB, alB);
  __syncthreads(); RESC(alB);
  finishSM(pB0, pB1, alB, l_reg, pa0, pa1, pa2, pa3); SBAR();
  pv_d0(o, vb0 + (int)SHM_V, pa0, pa1, pa2, pa3);
  if (hi == 0) li_l[r32] = l_reg;
  asm volatile("s_waitcnt lgkmcnt(0)" ::: "memory");
  float rli[16];
#pragma unroll
  for (int r = 0; r < 16; ++r) rli[r] = __builtin_amdgcn_rcpf(li_l[crow(r, hi)]);
  u16* Ow = Ob + (long)(wid * QBLK) * LDO;
#pragma unroll
  for (int r = 0; r < 16; ++r) { int orow = crow(r, hi);
    for (int d0 = 0; d0 < 4; ++d0) Ow[(long)orow * LDO + d0 * 32 + r32] = f2bf(o[d0][r] * rli[r]); }
#undef SLOAD
#undef SWRITE
#undef SWAIT
#undef RESC
#undef LD8
}

extern "C" void kernel_launch(void* const* d_in, const int* in_sizes, int n_in, void* d_out, int out_size, void* d_ws,
                              size_t ws_size, hipStream_t stream) {
  const u16* x = (const u16*)d_in[0];
  const u16* kc = (const u16*)d_in[1];
  const u16* vc = (const u16*)d_in[2];
  const u16* wq = (const u16*)d_in[3];
  const u16* wo = (const u16*)d_in[4];
  const u16* w_in = (const u16*)d_in[6];
  const u16* w_sp = (const u16*)d_in[7];
  const u16* b_sp = (const u16*)d_in[8];
  const u16* w_out = (const u16*)d_in[9];
  const u16* cvec = (const u16*)d_in[10];
  const u16* w_mod = (const u16*)d_in[11];
  u16* out = (u16*)d_out;

  char* ws = (char*)d_ws;
  size_t off = 0;
  auto alloc = [&](size_t bytes) { char* p = ws + off; off += (bytes + 255) & ~(size_t)255; return p; };
  float* mod = (float*)alloc(6 * DM * 4);
  u16* h1 = (u16*)alloc((size_t)SEQ * DM * 2);
  u16* q = (u16*)alloc((size_t)SEQ * NH * HD * 2);
  u16* o = (u16*)alloc((size_t)SEQ * NH * HD * 2);
  float* part = (float*)alloc((size_t)SPLITA * SEQ * DM * 4);
  u16* x1 = (u16*)alloc((size_t)SEQ * DM * 2);
  u16* h2 = (u16*)alloc((size_t)SEQ * DM * 2);
  u16* z1 = (u16*)alloc((size_t)SEQ * FH * 2);
  u16* z2 = (u16*)alloc((size_t)SEQ * FH * 2);
  u16* z2n = (u16*)alloc((size_t)SEQ * FH * 2);
  u16* u = (u16*)alloc((size_t)SEQ * FH * 2);

  (void)hipFuncSetAttribute((const void*)k_attn, hipFuncAttributeMaxDynamicSharedMemorySize, (int)SHM_ATTN);

  k_mod<<<dim3(6 * DM / 64), dim3(256), 0, stream>>>(cvec, w_mod, mod);
  k_ln_mod<<<dim3(SEQ / 4), dim3(256), 0, stream>>>(x, mod, h1);
  {
    GemmArgs g{h1, wq, q, nullptr, nullptr, nullptr, nullptr, DM, NH * HD, NH * HD, DM};
    launch_gemm<128, 2, 4, 4, EPI_BF16>(g, SEQ, NH * HD, 1, stream);
  }
  k_attn<<<dim3(256), dim3(512), SHM_ATTN, stream>>>(q, kc, vc, o);
  {
    GemmArgs g{o, wo, part, nullptr, nullptr, nullptr, nullptr, NH * HD, DM, DM, NH * HD / SPLITA};
    launch_gemm<128, 2, 4, 4, EPI_F32PART>(g, SEQ, DM, SPLITA, stream);
  }
  k_reduce_ln<<<dim3(SEQ / 4), dim3(256), 0, stream>>>(part, x, mod, x1, h2);
  {
    GemmArgs g{h2, w_in, z1, z2, nullptr, nullptr, nullptr, DM, FF, FH, DM};
    launch_gemm<128, 2, 4, 4, EPI_GELU>(g, SEQ, FF, 1, stream);
  }
  k_ln_z2<<<dim3(SEQ / 4), dim3(256), 0, stream>>>(z2, z2n);
  {
    GemmArgs g{w_sp, z2n, u, nullptr, b_sp, z1, nullptr, SEQ, FH, FH, SEQ};
    launch_gemm<64, 2, 4, 5, EPI_GATE>(g, SEQ, FH, 1, stream);
  }
  {
    GemmArgs g{u, w_out, out, nullptr, x1, nullptr, mod, FH, DM, DM, FH};
    launch_gemm<32, 1, 4, 4, EPI_FINAL>(g, SEQ, DM, 1, stream);
  }
}
```

```cpp
#include <hip/hip_runtime.h>
#include <hip/hip_bf16.h>
#include <stdint.h>


typedef unsigned short u16;
typedef short bf16x8 __attribute__((ext_vector_type(8)));
typedef short s16x4 __attribute__((ext_vector_type(4)));
typedef float f32x16 __attribute__((ext_vector_type(16)));
typedef float f32x4 __attribute__((ext_vector_type(4)));
typedef unsigned u32x4 __attribute__((ext_vector_type(4)));

constexpr int SEQ = 2048, DM = 512, NH = 32, HD = 128, SK = 2048, FH = 896, FF = 1792;
constexpr float LN_EPS = 1e-5f;

__device__ __forceinline__ u16 f2bf(float f) {
  unsigned u = __float_as_uint(f);
  u += 0x7fffu + ((u >> 16) & 1u);
  return (u16)(u >> 16);
}
__device__ __forceinline__ float bf2f(u16 b) { return __uint_as_float(((unsigned)b) << 16); }
__device__ __forceinline__ float rbf(float f) { return bf2f(f2bf(f)); }
__device__ __forceinline__ int crow(int r, int hi) { return (r & 3) + 8 * (r >> 2) + 4 * hi; }
__device__ __forceinline__ unsigned cvtpk(float lo, float hi) {
  unsigned r;
  asm volatile("v_cvt_pk_bf16_f32 %0, %1, %2" : "=v"(r) : "v"(lo), "v"(hi));
  return r;
}
#define SBAR() __builtin_amdgcn_sched_barrier(0)

__global__ __launch_bounds__(256) void k_mod(const u16* __restrict__ c, const u16* __restrict__ wmod,
                                             float* __restrict__ mod) {
  __shared__ float red[32][65];
  __shared__ float sc[DM];
  const int tid = threadIdx.x;
  for (int i = tid; i < DM; i += 256) {
    float x = bf2f(c[i]);
    sc[i] = rbf(x / (1.f + __expf(-x)));
  }
  __syncthreads();
  const int cc = tid & 7, kg = tid >> 3;
  const int j0 = blockIdx.x * 64 + cc * 8;
  bf16x8 w[16];
#pragma unroll
  for (int i = 0; i < 16; ++i) w[i] = *reinterpret_cast<const bf16x8*>(wmod + (size_t)(kg * 16 + i) * (6 * DM) + j0);
  float acc[8];
#pragma unroll
  for (int e = 0; e < 8; ++e) acc[e] = 0.f;
#pragma unroll
  for (int i = 0; i < 16; ++i) {
    const float sv = sc[kg * 16 + i];
#pragma unroll
    for (int e = 0; e < 8; ++e) acc[e] += sv * bf2f((u16)w[i][e]);
  }
#pragma unroll
  for (int e = 0; e < 8; ++e) red[kg][cc * 8 + e] = acc[e];
  __syncthreads();
  if (tid < 64) {
    float t = 0.f;
#pragma unroll
    for (int g = 0; g < 32; ++g) t += red[g][tid];
    mod[blockIdx.x * 64 + tid] = rbf(t);
  }
}

__device__ __forceinline__ float wave_sum(float v) {
#pragma unroll
  for (int o = 32; o >= 1; o >>= 1) v += __shfl_xor(v, o, 64);
  return v;
}

__global__ __launch_bounds__(256) void k_ln_mod(const u16* __restrict__ x, const float* __restrict__ mod,
                                                u16* __restrict__ h1) {
  const int wid = threadIdx.x >> 6, lane = threadIdx.x & 63;
  const int row = blockIdx.x * 4 + wid;
  const bf16x8 raw = *reinterpret_cast<const bf16x8*>(x + (size_t)row * DM + lane * 8);
  float v[8];
  float s = 0.f;
#pragma unroll
  for (int i = 0; i < 8; ++i) { v[i] = bf2f((u16)raw[i]); s += v[i]; }
  s = wave_sum(s);
  const float mean = s * (1.f / DM);
  float q = 0.f;
#pragma unroll
  for (int i = 0; i < 8; ++i) { float d = v[i] - mean; q += d * d; }
  q = wave_sum(q);
  const float mb = rbf(mean), vb = rbf(q * (1.f / DM));
  const float den = rbf(sqrtf(rbf(vb + LN_EPS)));
  bf16x8 o;
#pragma unroll
  for (int i = 0; i < 8; ++i) {
    const int cidx = lane * 8 + i;
    const float sa = mod[cidx], ca = mod[DM + cidx];
    float n = rbf(rbf(v[i] - mb) / den);
    float h = rbf(rbf(n * rbf(1.f + ca)) + sa);
    o[i] = (short)f2bf(h);
  }
  *reinterpret_cast<bf16x8*>(h1 + (size_t)row * DM + lane * 8) = o;
}

__device__ __forceinline__ int a_off(int row, int colB) { return row * 128 + (colB ^ (((row >> 1) & 7) << 4)); }
__device__ __forceinline__ int v_st(int k, int c) {
  const int kk = (k & ~0xC) | ((k & 4) << 1) | ((k & 8) >> 1);
  return ((kk >> 3) * 4 + (c >> 5)) * 512 + ((kk & 7) * 32 + (c & 31)) * 2;
}
__device__ __forceinline__ int v_rd_base(int lane) {
  return ((lane & 3) << 3) | (((lane >> 2) & 3) << 6) | (((lane >> 4) & 1) << 5) | (((lane >> 5) & 1) << 8);
}
constexpr int v_rd_off(int d0, int ks, int half) { return d0 * 512 + ks * 4096 + half * 2048; }
template <int OFF>
__device__ __forceinline__ s16x4 tr_read(int vb) {
  s16x4 r;
  asm volatile("ds_read_b64_tr_b16 %0, %1 offset:%2" : "=&v"(r) : "v"(vb), "i"(OFF) : "memory");
  return r;
}

enum { EPI_BF16 = 0, EPI_F32PART = 1, EPI_GELU = 2, EPI_GATE = 3, EPI_FINAL = 4 };

struct GemmArgs {
  const u16* A; const u16* B;
  void* C; void* C2;
  const u16* aux0; const u16* aux1; const float* mod;
  int lda, ldb, ldc, K;
  int mt, nt;
};

template <int OFFK>
__device__ __forceinline__ void b_frag(int vb, int j, s16x4& lo, s16x4& hi) {
  if (j == 0) { lo = tr_read<v_rd_off(0, OFFK, 0)>(vb); hi = tr_read<v_rd_off(0, OFFK, 1)>(vb); }
  else if (j == 1) { lo = tr_read<v_rd_off(1, OFFK, 0)>(vb); hi = tr_read<v_rd_off(1, OFFK, 1)>(vb); }
  else if (j == 2) { lo = tr_read<v_rd_off(2, OFFK, 0)>(vb); hi = tr_read<v_rd_off(2, OFFK, 1)>(vb); }
  else { lo = tr_read<v_rd_off(3, OFFK, 0)>(vb); hi = tr_read<v_rd_off(3, OFFK, 1)>(vb); }
}
template <int N> __device__ __forceinline__ void wait_vm() {
  if constexpr (N == 0) asm volatile("s_waitcnt vmcnt(0)" ::: "memory");
  else if constexpr (N == 1) asm volatile("s_waitcnt vmcnt(1)" ::: "memory");
  else if constexpr (N == 2) asm volatile("s_waitcnt vmcnt(2)" ::: "memory");
  else if constexpr (N == 3) asm volatile("s_waitcnt vmcnt(3)" ::: "memory");
  else if constexpr (N == 4) asm volatile("s_waitcnt vmcnt(4)" ::: "memory");
  else if constexpr (N == 5) asm volatile("s_waitcnt vmcnt(5)" ::: "memory");
  else if constexpr (N == 6) asm volatile("s_waitcnt vmcnt(6)" ::: "memory");
  else if constexpr (N == 8) asm volatile("s_waitcnt vmcnt(8)" ::: "memory");
  else if constexpr (N == 9) asm volatile("s_waitcnt vmcnt(9)" ::: "memory");
  else if constexpr (N == 10) asm volatile("s_waitcnt vmcnt(10)" ::: "memory");
  else if constexpr (N == 12) asm volatile("s_waitcnt vmcnt(12)" ::: "memory");
  else if constexpr (N == 15) asm volatile("s_waitcnt vmcnt(15)" ::: "memory");
  else static_assert(N < 0, "unsupported vmcnt");
}

template <int BM, int WM, int WN, int NS, int EPI, int MAP>
__global__ __launch_bounds__(64 * WM * WN) void k_gemm(GemmArgs g) {
  constexpr int BN = 128, BK = 64, NWV = WM * WN;
  constexpr int TM = BM / WM / 32, TN = BN / WN / 32;
  constexpr int STG = BM * 128 + 16384;
  constexpr int LPW = STG / 1024 / NWV;
  static_assert(LPW * 1024 * NWV == STG, "stage must split evenly over waves");
  extern __shared__ __attribute__((aligned(16))) char lds[];
  const int tid = threadIdx.x, wid = tid >> 6, lane = tid & 63, r32 = lane & 31, hi = lane >> 5;
  const int wm = wid / WN, wn = wid % WN;
  const int xcd = blockIdx.x & 7, loc = blockIdx.x >> 3;
  int tmi, tni, tsp = 0;
  if constexpr (MAP == 0) {
    tmi = xcd * (g.mt >> 3) + loc / g.nt; tni = loc % g.nt;
  } else if constexpr (MAP == 1) {
    tsp = xcd & 3; tmi = (xcd >> 2) * (g.mt >> 1) + loc / g.nt; tni = loc % g.nt;
  } else {
    tni = xcd * (g.nt >> 3) + loc % (g.nt >> 3); tmi = loc / (g.nt >> 3);
  }
  const int bm = tmi * BM, bn = tni * BN;
  const int kbase = tsp * g.K;
  const u16* Ab = g.A + (size_t)bm * g.lda + kbase;
  const u16* Bb = g.B + (size_t)kbase * g.ldb + bn;
  const u16* src[LPW];
#pragma unroll
  for (int i = 0; i < LPW; ++i) {
    const int p = (i * NWV + wid) * 1024 + lane * 16;
    if (p < BM * 128) {
      const int ci = p >> 4, row = ci >> 3, slot = ci & 7, cch = slot ^ ((row >> 1) & 7);
      src[i] = Ab + (size_t)row * g.lda + cch * 8;
    } else {
      const int q = p - BM * 128, st = q >> 9, rin = (q >> 6) & 7, cch = (q >> 4) & 3;
      const int kk = (st >> 2) * 8 + rin, c = (st & 3) * 32 + cch * 8;
      const int k = (kk & ~0xC) | ((kk & 4) << 1) | ((kk & 8) >> 1);
      src[i] = Bb + (size_t)k * g.ldb + c;
    }
  }
  auto stage = [&](int kt) {
    char* dst = lds + (kt % NS) * STG;
#pragma unroll
    for (int i = 0; i < LPW; ++i) {
      const int p = (i * NWV + wid) * 1024;
      const u16* sp = src[i] + ((p < BM * 128) ? (size_t)kt * BK : (size_t)kt * BK * g.ldb);
      __builtin_amdgcn_global_load_lds((const unsigned*)sp, (unsigned*)(dst + p), 16, 0, 0);
    }
  };
  f32x16 acc[TM][TN];
#pragma unroll
  for (int i = 0; i < TM; ++i)
#pragma unroll
    for (int j = 0; j < TN; ++j) acc[i][j] = f32x16{};

  const int nk = g.K / BK;
#pragma unroll
  for (int s = 0; s < NS - 1; ++s)
    if (s < nk) stage(s);
  const int vbB = (int)(uintptr_t)lds + BM * 128 + v_rd_base(lane) + wn * TN * 512;
  for (int kt = 0; kt < nk; ++kt) {
    const int rem = nk - 1 - kt;
    if (rem >= NS - 2) wait_vm<LPW*(NS - 2)>();
    else if (NS > 3 && rem == 1) wait_vm<LPW>();
    else wait_vm<0>();
    asm volatile("s_waitcnt lgkmcnt(0)" ::: "memory");
    __builtin_amdgcn_s_barrier();
    if (kt + NS - 1 < nk) stage(kt + NS - 1);
    const char* Ac = lds + (kt % NS) * STG;
    const int vb = vbB + (kt % NS) * STG;
#pragma unroll
    for (int ks = 0; ks < 4; ++ks) {
      bf16x8 af[TM];
#pragma unroll
      for (int i = 0; i < TM; ++i)
        af[i] = *reinterpret_cast<const bf16x8*>(Ac + a_off(wm * (BM / WM) + i * 32 + r32, (ks * 16 + hi * 8) * 2));
      s16x4 bl[TN], bh[TN];
#pragma unroll
      for (int j = 0; j < TN; ++j) {
        if (ks == 0) b_frag<0>(vb, j, bl[j], bh[j]);
        else if (ks == 1) b_frag<1>(vb, j, bl[j], bh[j]);
        else if (ks == 2) b_frag<2>(vb, j, bl[j], bh[j]);
        else b_frag<3>(vb, j, bl[j], bh[j]);
      }
      asm volatile("s_waitcnt lgkmcnt(0)" ::: "memory");
      SBAR();
#pragma unroll
      for (int j = 0; j < TN; ++j) {
        const bf16x8 bfr = {bl[j][0], bl[j][1], bl[j][2], bl[j][3], bh[j][0], bh[j][1], bh[j][2], bh[j][3]};
#pragma unroll
        for (int i = 0; i < TM; ++i) acc[i][j] = __builtin_amdgcn_mfma_f32_32x32x16_bf16(af[i], bfr, acc[i][j], 0, 0, 0);
      }
    }
  }
#pragma unroll
  for (int i = 0; i < TM; ++i)
#pragma unroll
    for (int j = 0; j < TN; ++j) {
      const int col = bn + wn * (BN / WN) + j * 32 + r32;
#pragma unroll
      for (int r = 0; r < 16; ++r) {
        const int row = bm + wm * (BM / WM) + i * 32 + crow(r, hi);
        const float v = acc[i][j][r];
        if constexpr (EPI == EPI_BF16) {
          ((u16*)g.C)[(size_t)row * g.ldc + col] = f2bf(v);
        } else if constexpr (EPI == EPI_F32PART) {
          ((float*)g.C)[((size_t)tsp * SEQ + row) * g.ldc + col] = v;
        } else if constexpr (EPI == EPI_GELU) {
          const float xb = rbf(v);
          const float t = 0.7978845608028654f * (xb + 0.044715f * xb * xb * xb);
          const float ge = 0.5f * xb * (1.f + tanhf(t));
          if (col < FH) ((u16*)g.C)[(size_t)row * FH + col] = f2bf(ge);
          else ((u16*)g.C2)[(size_t)row * FH + (col - FH)] = f2bf(ge);
        } else if constexpr (EPI == EPI_GATE) {
          const float gg = rbf(rbf(v) + bf2f(g.aux0[row]));
          const float z1 = bf2f(g.aux1[(size_t)row * FH + col]);
          ((u16*)g.C)[(size_t)row * FH + col] = f2bf(z1 * gg);
        } else {
          const float o = rbf(v);
          const float gb = g.mod[5 * DM + col];
          const float x1 = bf2f(g.aux0[(size_t)row * DM + col]);
          ((u16*)g.C)[(size_t)row * DM + col] = f2bf(x1 + rbf(gb * o));
        }
      }
    }
}
template <int BM, int WM, int WN, int NS, int EPI, int MAP>
static void launch_gemm(GemmArgs g, int M, int N, int splits, hipStream_t stream) {
  constexpr int shm = NS * (BM * 128 + 16384);
  g.mt = M / BM; g.nt = N / 128;
  (void)hipFuncSetAttribute((const void*)k_gemm<BM, WM, WN, NS, EPI, MAP>, hipFuncAttributeMaxDynamicSharedMemorySize, shm);
  k_gemm<BM, WM, WN, NS, EPI, MAP><<<dim3(g.mt * g.nt * splits), dim3(64 * WM * WN), shm, stream>>>(g);
}

constexpr int SPLITA = 4;
__global__ __launch_bounds__(256) void k_reduce_ln(const float* __restrict__ part, const u16* __restrict__ x,
                                                   const float* __restrict__ mod, u16* __restrict__ x1,
                                                   u16* __restrict__ h2) {
  const int wid = threadIdx.x >> 6, lane = threadIdx.x & 63;
  const int row = blockIdx.x * 4 + wid;
  const bf16x8 raw = *reinterpret_cast<const bf16x8*>(x + (size_t)row * DM + lane * 8);
  float a[8];
#pragma unroll
  for (int i = 0; i < 8; ++i) a[i] = 0.f;
#pragma unroll
  for (int s = 0; s < SPLITA; ++s) {
    const f32x4* p = reinterpret_cast<const f32x4*>(part + ((size_t)s * SEQ + row) * DM + lane * 8);
    f32x4 p0 = p[0], p1 = p[1];
#pragma unroll
    for (int i = 0; i < 4; ++i) { a[i] += p0[i]; a[4 + i] += p1[i]; }
  }
  float v[8];
  float s = 0.f;
#pragma unroll
  for (int i = 0; i < 8; ++i) {
    const int cidx = lane * 8 + i;
    const float ga = mod[2 * DM + cidx];
    v[i] = rbf(bf2f((u16)raw[i]) + rbf(ga * rbf(a[i])));
    s += v[i];
  }
  s = wave_sum(s);
  const float mean = s * (1.f / DM);
  float q = 0.f;
#pragma unroll
  for (int i = 0; i < 8; ++i) { float d = v[i] - mean; q += d * d; }
  q = wave_sum(q);
  const float mb = rbf(mean), vb = rbf(q * (1.f / DM));
  const float den = rbf(sqrtf(rbf(vb + LN_EPS)));
  bf16x8 o, ox;
#pragma unroll
  for (int i = 0; i < 8; ++i) {
    const int cidx = lane * 8 + i;
    const float sb = mod[3 * DM + cidx], cb = mod[4 * DM + cidx];
    float n = rbf(rbf(v[i] - mb) / den);
    float h = rbf(rbf(n * rbf(1.f + cb)) + sb);
    o[i] = (short)f2bf(h);
    ox[i] = (short)f2bf(v[i]);
  }
  *reinterpret_cast<bf16x8*>(h2 + (size_t)row * DM + lane * 8) = o;
  *reinterpret_cast<bf16x8*>(x1 + (size_t)row * DM + lane * 8) = ox;
}

__global__ __launch_bounds__(256) void k_ln_z2(const u16* __restrict__ z2, u16* __restrict__ z2n) {
  const int wid = threadIdx.x >> 6, lane = threadIdx.x & 63;
  const int row = blockIdx.x * 4 + wid;
  const unsigned* p = reinterpret_cast<const unsigned*>(z2 + (size_t)row * FH);
  float v[14];
  float s = 0.f;
#pragma unroll
  for (int i = 0; i < 7; ++i) {
    unsigned w = p[i * 64 + lane];
    v[2 * i] = bf2f((u16)(w & 0xffff));
    v[2 * i + 1] = bf2f((u16)(w >> 16));
    s += v[2 * i] + v[2 * i + 1];
  }
  s = wave_sum(s);
  const float mean = s * (1.f / FH);
  float q = 0.f;
#pragma unroll
  for (int i = 0; i < 14; ++i) { float d = v[i] - mean; q += d * d; }
  q = wave_sum(q);
  const float mb = rbf(mean), vb = rbf(q * (1.f / FH));
  const float den = rbf(sqrtf(rbf(vb + LN_EPS)));
  unsigned* o = reinterpret_cast<unsigned*>(z2n + (size_t)row * FH);
#pragma unroll
  for (int i = 0; i < 7; ++i) {
    const u16 a = f2bf(rbf(v[2 * i] - mb) / den), b = f2bf(rbf(v[2 * i + 1] - mb) / den);
    o[i * 64 + lane] = (unsigned)a | ((unsigned)b << 16);
  }
}

constexpr int NW = 8, QBLK = 32, KVBLK = 64;
constexpr float SCALE = 0.088388347648318440f;
constexpr float THR = 8.f;
constexpr int LDQ = NH * HD, LDK = HD, LDO = NH * HD;
constexpr size_t SHM_V = KVBLK * HD * 2, SHM_K = KVBLK * HD * 2, SHM_ATTN = 2 * SHM_V + 2 * SHM_K + NW * 64 * 4;
#define KSWZ(row, colB) ((row) * 256 + ((colB) ^ (((row) & 7) << 4)))

__device__ __forceinline__ void partialSM(f32x16& p0, f32x16& p1, float& m_reg, float& mn, float& alpha) {
  constexpr float C = SCALE * 1.4426950408889634f;
  float pmax = p0[0];
  for (int r = 1; r < 16; ++r) pmax = fmaxf(pmax, p0[r]);
  for (int r = 0; r < 16; ++r) pmax = fmaxf(pmax, p1[r]);
  { auto rr = __builtin_amdgcn_permlane32_swap(__float_as_uint(pmax), __float_as_uint(pmax), false, false);
    pmax = fmaxf(__uint_as_float(rr[0]), __uint_as_float(rr[1])); }
  if (__builtin_expect(__all(pmax - m_reg <= THR / SCALE), 1)) { mn = m_reg; alpha = 1.f; }
  else { mn = fmaxf(m_reg, pmax); alpha = __builtin_amdgcn_exp2f((m_reg - mn) * C); m_reg = mn; }
  float mnC = -mn * C;
  for (int r = 0; r < 16; ++r) p0[r] = fmaf(p0[r], C, mnC);
  for (int r = 0; r < 16; ++r) p1[r] = fmaf(p1[r], C, mnC);
  for (int r = 0; r < 16; ++r) p0[r] = __builtin_amdgcn_exp2f(p0[r]);
}
__device__ __forceinline__ void finishSM(f32x16& p0, f32x16& p1, float alpha, float& l_reg, bf16x8& pa0, bf16x8& pa1,
                                         bf16x8& pa2, bf16x8& pa3) {
  for (int r = 0; r < 16; ++r) p1[r] = __builtin_amdgcn_exp2f(p1[r]);
  float ps = 0;
  for (int r = 0; r < 16; ++r) ps += p0[r];
  for (int r = 0; r < 16; ++r) ps += p1[r];
  { auto rr = __builtin_amdgcn_permlane32_swap(__float_as_uint(ps), __float_as_uint(ps), false, false);
    ps = __uint_as_float(rr[0]) + __uint_as_float(rr[1]); }
  l_reg = l_reg * alpha + ps;
#define PK4(P, BASE, OUT) do { unsigned a0 = cvtpk(P[BASE + 0], P[BASE + 1]), a1 = cvtpk(P[BASE + 2], P[BASE + 3]);   \
    unsigned b0 = cvtpk(P[BASE + 4], P[BASE + 5]), b1 = cvtpk(P[BASE + 6], P[BASE + 7]);                              \
    auto r0 = __builtin_amdgcn_permlane32_swap(a0, b0, false, false); auto r1 = __builtin_amdgcn_permlane32_swap(a1, b1, false, false); \
    u32x4 w = {r0[0], r1[0], r0[1], r1[1]}; OUT = *reinterpret_cast<bf16x8*>(&w); } while (0)
  PK4(p0, 0, pa0); PK4(p0, 8, pa1); PK4(p1, 0, pa2); PK4(p1, 8, pa3);
#undef PK4
}
__device__ __forceinline__ void qkt(f32x16& p0, f32x16& p1, const char* Ks, const bf16x8* qr, int r32, int hi) {
  p0 = f32x16{}; p1 = f32x16{};
  for (int d0 = 0; d0 < 8; ++d0) { int cb = (d0 * 16 + hi * 8) * 2;
    bf16x8 b0 = *reinterpret_cast<const bf16x8*>(Ks + KSWZ(r32, cb));
    bf16x8 b1 = *reinterpret_cast<const bf16x8*>(Ks + KSWZ(32 + r32, cb));
    p0 = __builtin_amdgcn_mfma_f32_32x32x16_bf16(b0, qr[d0], p0, 0, 0, 0);
    p1 = __builtin_amdgcn_mfma_f32_32x32x16_bf16(b1, qr[d0], p1, 0, 0, 0); }
}
template <int D0>
__device__ __forceinline__ void pv_one(f32x16& od, int vb, bf16x8 pa0, bf16x8 pa1, bf16x8 pa2, bf16x8 pa3) {
  const s16x4 l0 = tr_read<v_rd_off(D0, 0, 0)>(vb), h0 = tr_read<v_rd_off(D0, 0, 1)>(vb), l1 = tr_read<v_rd_off(D0, 1, 0)>(vb), h1 = tr_read<v_rd_off(D0, 1, 1)>(vb);
  const s16x4 l2 = tr_read<v_rd_off(D0, 2, 0)>(vb), h2 = tr_read<v_rd_off(D0, 2, 1)>(vb), l3 = tr_read<v_rd_off(D0, 3, 0)>(vb), h3 = tr_read<v_rd_off(D0, 3, 1)>(vb);
  asm volatile("s_waitcnt lgkmcnt(0)" ::: "memory"); SBAR();
#define PK(L, H) (bf16x8){L[0], L[1], L[2], L[3], H[0], H[1], H[2], H[3]}
  od = __builtin_amdgcn_mfma_f32_32x32x16_bf16(pa0, PK(l0, h0), od, 0, 0, 0);
  od = __builtin_amdgcn_mfma_f32_32x32x16_bf16(pa1, PK(l1, h1), od, 0, 0, 0);
  od = __builtin_amdgcn_mfma_f32_32x32x16_bf16(pa2, PK(l2, h2), od, 0, 0, 0);
  od = __builtin_amdgcn_mfma_f32_32x32x16_bf16(pa3, PK(l3, h3), od, 0, 0, 0);
#undef PK
}
__device__ __forceinline__ void pv_d0(f32x16* o, int vb, bf16x8 pa0, bf16x8 pa1, bf16x8 pa2, bf16x8 pa3) {
  pv_one<0>(o[0], vb, pa0, pa1, pa2, pa3); pv_one<1>(o[1], vb, pa0, pa1, pa2, pa3);
  pv_one<2>(o[2], vb, pa0, pa1, pa2, pa3); pv_one<3>(o[3], vb, pa0, pa1, pa2, pa3);
}

__global__ __launch_bounds__(64 * NW, 1) void k_attn(const u16* __restrict__ Q, const u16* __restrict__ Kc,
                                                     const u16* __restrict__ Vc, u16* __restrict__ O) {
  extern __shared__ __attribute__((aligned(16))) char lds[];
  const int bid = blockIdx.x;
  const int h = (bid & 7) + 8 * (bid >> 6);
  const long i0 = (long)((bid >> 3) & 7) * (QBLK * NW);
  const u16* Qb = Q + i0 * LDQ + h * HD;
  const u16* Kh = Kc + (long)h * SK * HD;
  const u16* Vh = Vc + (long)h * SK * HD;
  u16* Ob = O + i0 * LDO + h * HD;
  const int tid = threadIdx.x, wid = tid >> 6, lane = tid & 63, r32 = lane & 31, hi = lane >> 5;
  char* V_lds = lds; char* K_lds = lds + 2 * SHM_V;
  float* ws = (float*)(lds + 2 * SHM_V + 2 * SHM_K) + wid * 64; float* li_l = ws; float* al_l = ws + 32;
  float m_reg = -1e30f, l_reg = 0; f32x16 o[4] = {}; bf16x8 qr[8];
  const u16* Qw = Qb + (long)(wid * QBLK + r32) * LDQ + hi * 8;
#pragma unroll
  for (int d0 = 0; d0 < 8; ++d0) qr[d0] = *reinterpret_cast<const bf16x8*>(Qw + d0 * 16);
  const int sr = tid >> 4, sc = (tid & 15) * 8, vst0 = v_st(sr, sc), vst1 = v_st(32 + sr, sc);
  const int vb0 = (int)(uintptr_t)V_lds + v_rd_base(lane);
  struct { bf16x8 vs0, vs1, ks0, ks1; } sr_[2];
#define LD8(p) (*reinterpret_cast<const bf16x8*>(p))
#define SLOAD(i, k0) do { sr_[i].vs0 = LD8(&Vh[(long)((k0) + sr) * LDK + sc]); sr_[i].vs1 = LD8(&Vh[(long)((k0) + 32 + sr) * LDK + sc]); \
    sr_[i].ks0 = LD8(&Kh[(long)((k0) + sr) * LDK + sc]); sr_[i].ks1 = LD8(&Kh[(long)((k0) + 32 + sr) * LDK + sc]); } while (0)
#define SWRITE(b, i) do { *(bf16x8*)(V_lds + (b) * SHM_V + vst0) = sr_[i].vs0;          \
    *(bf16x8*)(V_lds + (b) * SHM_V + vst1) = sr_[i].vs1; int kc = sc * 2;               \
    *(bf16x8*)(K_lds + (b) * SHM_K + KSWZ(sr, kc)) = sr_[i].ks0;                       \
    *(bf16x8*)(K_lds + (b) * SHM_K + KSWZ(32 + sr, kc)) = sr_[i].ks1; } while (0)
#define SWAIT() asm volatile("s_waitcnt vmcnt(4)" ::: "memory")
#define RESC(a) do { if (__any((a) < 1.f)) { if (hi == 0) al_l[r32] = (a); asm volatile("s_waitcnt lgkmcnt(0)" ::: "memory"); \
    for (int d = 0; d < 4; ++d) for (int r = 0; r < 16; ++r) o[d][r] *= al_l[crow(r, hi)]; } } while (0)
  f32x16 pA0, pA1, pB0, pB1; float mnA, mnB, alA, alB; bf16x8 pa0, pa1, pa2, pa3; const int NT = SK / KVBLK;
  constexpr int SE = 0, SO = 1;
  SLOAD(SE, 0); asm volatile("s_waitcnt vmcnt(0)" ::: "memory"); SWRITE(0, SE); __syncthreads();
  qkt(pA0, pA1, K_lds, qr, r32, hi); partialSM(pA0, pA1, m_reg, mnA, alA);
  SLOAD(SO, KVBLK); SLOAD(SE, 2 * KVBLK);
  SWAIT(); SWRITE(1, SO); __syncthreads();
  for (int j = 1; j + 1 < NT; j += 2) {
    SBAR(); qkt(pB0, pB1, K_lds + SHM_K, qr, r32, hi);
    finishSM(pA0, pA1, alA, l_reg, pa0, pa1, pa2, pa3); SBAR();
    SLOAD(SO, (j + 2) * KVBLK); SBAR();
    pv_d0(o, vb0, pa0, pa1, pa2, pa3); partialSM(pB0, pB1, m_reg, mnB, alB);
    __syncthreads(); SWAIT(); SWRITE(0, SE);
    RESC(alB); __syncthreads();
    SBAR(); qkt(pA0, pA1, K_lds, qr, r32, hi);
    finishSM(pB0, pB1, alB, l_reg, pa0, pa1, pa2, pa3); SBAR();
    if (j + 3 < NT) SLOAD(SE, (j + 3) * KVBLK); SBAR();
    pv_d0(o, vb0 + (int)SHM_V, pa0, pa1, pa2, pa3); partialSM(pA0, pA1, m_reg, mnA, alA);
    __syncthreads(); SWAIT(); SWRITE(1, SO);
    RESC(alA); __syncthreads();
  }
  SBAR(); qkt(pB0, pB1, K_lds + SHM_K, qr, r32, hi);
  finishSM(pA0, pA1, alA, l_reg, pa0, pa1, pa2, pa3); SBAR();
  pv_d0(o, vb0, pa0, pa1, pa2, pa3); partialSM(pB0, pB1, m_reg, mnB, alB);
  __syncthreads(); RESC(alB);
  finishSM(pB0, pB1, alB, l_reg, pa0, pa1, pa2, pa3); SBAR();
  pv_d0(o, vb0 + (int)SHM_V, pa0, pa1, pa2, pa3);
  if (hi == 0) li_l[r32] = l_reg;
  asm volatile("s_waitcnt lgkmcnt(0)" ::: "memory");
  float rli[16];
#pragma unroll
  for (int r = 0; r < 16; ++r) rli[r] = __builtin_amdgcn_rcpf(li_l[crow(r, hi)]);
  u16* Ow = Ob + (long)(wid * QBLK) * LDO;
#pragma unroll
  for (int r = 0; r < 16; ++r) { int orow = crow(r, hi);
    for (int d0 = 0; d0 < 4; ++d0) Ow[(long)orow * LDO + d0 * 32 + r32] = f2bf(o[d0][r] * rli[r]); }
#undef SLOAD
#undef SWRITE
#undef SWAIT
#undef RESC
#undef LD8
}

extern "C" void kernel_launch(void* const* d_in, const int* in_sizes, int n_in, void* d_out, int out_size, void* d_ws,
                              size_t ws_size, hipStream_t stream) {
  const u16* x = (const u16*)d_in[0];
  const u16* kc = (const u16*)d_in[1];
  const u16* vc = (const u16*)d_in[2];
  const u16* wq = (const u16*)d_in[3];
  const u16* wo = (const u16*)d_in[4];
  const u16* w_in = (const u16*)d_in[6];
  const u16* w_sp = (const u16*)d_in[7];
  const u16* b_sp = (const u16*)d_in[8];
  const u16* w_out = (const u16*)d_in[9];
  const u16* cvec = (const u16*)d_in[10];
  const u16* w_mod = (const u16*)d_in[11];
  u16* out = (u16*)d_out;

  char* ws = (char*)d_ws;
  size_t off = 0;
  auto alloc = [&](size_t bytes) { char* p = ws + off; off += (bytes + 255) & ~(size_t)255; return p; };
  float* mod = (float*)alloc(6 * DM * 4);
  u16* h1 = (u16*)alloc((size_t)SEQ * DM * 2);
  u16* q = (u16*)alloc((size_t)SEQ * NH * HD * 2);
  u16* o = (u16*)alloc((size_t)SEQ * NH * HD * 2);
  float* part = (float*)alloc((size_t)SPLITA * SEQ * DM * 4);
  u16* x1 = (u16*)alloc((size_t)SEQ * DM * 2);
  u16* h2 = (u16*)alloc((size_t)SEQ * DM * 2);
  u16* z1 = (u16*)alloc((size_t)SEQ * FH * 2);
  u16* z2 = (u16*)alloc((size_t)SEQ * FH * 2);
  u16* z2n = (u16*)alloc((size_t)SEQ * FH * 2);
  u16* u = (u16*)alloc((size_t)SEQ * FH * 2);

  (void)hipFuncSetAttribute((const void*)k_attn, hipFuncAttributeMaxDynamicSharedMemorySize, (int)SHM_ATTN);

  k_mod<<<dim3(6 * DM / 64), dim3(256), 0, stream>>>(cvec, w_mod, mod);
  k_ln_mod<<<dim3(SEQ / 4), dim3(256), 0, stream>>>(x, mod, h1);
  {
    GemmArgs g{h1, wq, q, nullptr, nullptr, nullptr, nullptr, DM, NH * HD, NH * HD, DM};
    launch_gemm<128, 2, 4, 4, EPI_BF16, 2>(g, SEQ, NH * HD, 1, stream);
  }
  k_attn<<<dim3(256), dim3(512), SHM_ATTN, stream>>>(q, kc, vc, o);
  {
    GemmArgs g{o, wo, part, nullptr, nullptr, nullptr, nullptr, NH * HD, DM, DM, NH * HD / SPLITA};
    launch_gemm<128, 2, 4, 4, EPI_F32PART, 1>(g, SEQ, DM, SPLITA, stream);
  }
  k_reduce_ln<<<dim3(SEQ / 4), dim3(256), 0, stream>>>(part, x, mod, x1, h2);
  {
    GemmArgs g{h2, w_in, z1, z2, nullptr, nullptr, nullptr, DM, FF, FH, DM};
    launch_gemm<128, 2, 4, 4, EPI_GELU, 0>(g, SEQ, FF, 1, stream);
  }
  k_ln_z2<<<dim3(SEQ / 4), dim3(256), 0, stream>>>(z2, z2n);
  {
    GemmArgs g{w_sp, z2n, u, nullptr, b_sp, z1, nullptr, SEQ, FH, FH, SEQ};
    launch_gemm<64, 2, 4, 5, EPI_GATE, 0>(g, SEQ, FH, 1, stream);
  }
  {
    GemmArgs g{u, w_out, out, nullptr, x1, nullptr, mod, FH, DM, DM, FH};
    launch_gemm<32, 1, 4, 4, EPI_FINAL, 0>(g, SEQ, DM, 1, stream);
  }
}
```

```cpp
#include <hip/hip_runtime.h>
#include <hip/hip_bf16.h>
#include <stdint.h>

typedef unsigned short u16;
typedef short bf16x8 __attribute__((ext_vector_type(8)));
typedef short s16x4 __attribute__((ext_vector_type(4)));
typedef float f32x16 __attribute__((ext_vector_type(16)));
typedef float f32x4 __attribute__((ext_vector_type(4)));
typedef unsigned u32x4 __attribute__((ext_vector_type(4)));

constexpr int SEQ = 2048, DM = 512, NH = 32, HD = 128, SK = 2048, FH = 896, FF = 1792;
constexpr float LN_EPS = 1e-5f;

__device__ __forceinline__ u16 f2bf(float f) {
  unsigned u = __float_as_uint(f);
  u += 0x7fffu + ((u >> 16) & 1u);
  return (u16)(u >> 16);
}
__device__ __forceinline__ float bf2f(u16 b) { return __uint_as_float(((unsigned)b) << 16); }
__device__ __forceinline__ float rbf(float f) { return bf2f(f2bf(f)); }
__device__ __forceinline__ int crow(int r, int hi) { return (r & 3) + 8 * (r >> 2) + 4 * hi; }
__device__ __forceinline__ unsigned cvtpk(float lo, float hi) {
  unsigned r;
  asm volatile("v_cvt_pk_bf16_f32 %0, %1, %2" : "=v"(r) : "v"(lo), "v"(hi));
  return r;
}
#define SBAR() __builtin_amdgcn_sched_barrier(0)

__global__ __launch_bounds__(256) void k_mod(const u16* __restrict__ c, const u16* __restrict__ wmod,
                                             float* __restrict__ mod) {
  __shared__ float red[32][65];
  __shared__ float sc[DM];
  const int tid = threadIdx.x;
  for (int i = tid; i < DM; i += 256) {
    float x = bf2f(c[i]);
    sc[i] = rbf(x / (1.f + __expf(-x)));
  }
  __syncthreads();
  const int cc = tid & 7, kg = tid >> 3;
  const int j0 = blockIdx.x * 64 + cc * 8;
  bf16x8 w[16];
#pragma unroll
  for (int i = 0; i < 16; ++i) w[i] = *reinterpret_cast<const bf16x8*>(wmod + (size_t)(kg * 16 + i) * (6 * DM) + j0);
  float acc[8];
#pragma unroll
  for (int e = 0; e < 8; ++e) acc[e] = 0.f;
#pragma unroll
  for (int i = 0; i < 16; ++i) {
    const float sv = sc[kg * 16 + i];
#pragma unroll
    for (int e = 0; e < 8; ++e) acc[e] += sv * bf2f((u16)w[i][e]);
  }
#pragma unroll
  for (int e = 0; e < 8; ++e) red[kg][cc * 8 + e] = acc[e];
  __syncthreads();
  if (tid < 64) {
    float t = 0.f;
#pragma unroll
    for (int g = 0; g < 32; ++g) t += red[g][tid];
    mod[blockIdx.x * 64 + tid] = rbf(t);
  }
}

__device__ __forceinline__ float wave_sum(float v) {
#pragma unroll
  for (int o = 32; o >= 1; o >>= 1) v += __shfl_xor(v, o, 64);
  return v;
}

__global__ __launch_bounds__(256) void k_ln_mod(const u16* __restrict__ x, const float* __restrict__ mod,
                                                u16* __restrict__ h1) {
  const int wid = threadIdx.x >> 6, lane = threadIdx.x & 63;
  const int row = blockIdx.x * 4 + wid;
  const bf16x8 raw = *reinterpret_cast<const bf16x8*>(x + (size_t)row * DM + lane * 8);
  float v[8];
  float s = 0.f;
#pragma unroll
  for (int i = 0; i < 8; ++i) { v[i] = bf2f((u16)raw[i]); s += v[i]; }
  s = wave_sum(s);
  const float mean = s * (1.f / DM);
  float q = 0.f;
#pragma unroll
  for (int i = 0; i < 8; ++i) { float d = v[i] - mean; q += d * d; }
  q = wave_sum(q);
  const float mb = rbf(mean), vb = rbf(q * (1.f / DM));
  const float den = rbf(sqrtf(rbf(vb + LN_EPS)));
  bf16x8 o;
#pragma unroll
  for (int i = 0; i < 8; ++i) {
    const int cidx = lane * 8 + i;
    const float sa = mod[cidx], ca = mod[DM + cidx];
    float n = rbf(rbf(v[i] - mb) / den);
    float h = rbf(rbf(n * rbf(1.f + ca)) + sa);
    o[i] = (short)f2bf(h);
  }
  *reinterpret_cast<bf16x8*>(h1 + (size_t)row * DM + lane * 8) = o;
}

__device__ __forceinline__ int a_off(int row, int colB) { return row * 128 + (colB ^ (((row >> 1) & 7) << 4)); }
__device__ __forceinline__ int v_st(int k, int c) {
  const int kk = (k & ~0xC) | ((k & 4) << 1) | ((k & 8) >> 1);
  return ((kk >> 3) * 4 + (c >> 5)) * 512 + ((kk & 7) * 32 + (c & 31)) * 2;
}
__device__ __forceinline__ int v_rd_base(int lane) {
  return ((lane & 3) << 3) | (((lane >> 2) & 3) << 6) | (((lane >> 4) & 1) << 5) | (((lane >> 5) & 1) << 8);
}
constexpr int v_rd_off(int d0, int ks, int half) { return d0 * 512 + ks * 4096 + half * 2048; }
template <int OFF>
__device__ __forceinline__ s16x4 tr_read(int vb) {
  s16x4 r;
  asm volatile("ds_read_b64_tr_b16 %0, %1 offset:%2" : "=&v"(r) : "v"(vb), "i"(OFF) : "memory");
  return r;
}

enum { EPI_BF16 = 0, EPI_F32PART = 1, EPI_GELU = 2, EPI_GATE = 3, EPI_FINAL = 4 };

struct GemmArgs {
  const u16* A; const u16* B;
  void* C; void* C2;
  const u16* aux0; const u16* aux1; const float* mod;
  int lda, ldb, ldc, K;
  int mt, nt;
};

template <int OFFK>
__device__ __forceinline__ void b_frag(int vb, int j, s16x4& lo, s16x4& hi) {
  if (j == 0) { lo = tr_read<v_rd_off(0, OFFK, 0)>(vb); hi = tr_read<v_rd_off(0, OFFK, 1)>(vb); }
  else if (j == 1) { lo = tr_read<v_rd_off(1, OFFK, 0)>(vb); hi = tr_read<v_rd_off(1, OFFK, 1)>(vb); }
  else if (j == 2) { lo = tr_read<v_rd_off(2, OFFK, 0)>(vb); hi = tr_read<v_rd_off(2, OFFK, 1)>(vb); }
  else { lo = tr_read<v_rd_off(3, OFFK, 0)>(vb); hi = tr_read<v_rd_off(3, OFFK, 1)>(vb); }
}
template <int N> __device__ __forceinline__ void wait_vm() {
  if constexpr (N == 0) asm volatile("s_waitcnt vmcnt(0)" ::: "memory");
  else if constexpr (N == 1) asm volatile("s_waitcnt vmcnt(1)" ::: "memory");
  else if constexpr (N == 2) asm volatile("s_waitcnt vmcnt(2)" ::: "memory");
  else if constexpr (N == 3) asm volatile("s_waitcnt vmcnt(3)" ::: "memory");
  else if constexpr (N == 4) asm volatile("s_waitcnt vmcnt(4)" ::: "memory");
  else if constexpr (N == 5) asm volatile("s_waitcnt vmcnt(5)" ::: "memory");
  else if constexpr (N == 6) asm volatile("s_waitcnt vmcnt(6)" ::: "memory");
  else if constexpr (N == 8) asm volatile("s_waitcnt vmcnt(8)" ::: "memory");
  else if constexpr (N == 9) asm volatile("s_waitcnt vmcnt(9)" ::: "memory");
  else if constexpr (N == 10) asm volatile("s_waitcnt vmcnt(10)" ::: "memory");
  else if constexpr (N == 12) asm volatile("s_waitcnt vmcnt(12)" ::: "memory");
  else if constexpr (N == 15) asm volatile("s_waitcnt vmcnt(15)" ::: "memory");
  else static_assert(N < 0, "unsupported vmcnt");
}

template <int BM, int WM, int WN, int NS, int EPI, int MAP>
__global__ __launch_bounds__(64 * WM * WN) void k_gemm(GemmArgs g) {
  constexpr int BN = 128, BK = 64, NWV = WM * WN;
  constexpr int TM = BM / WM / 32, TN = BN / WN / 32;
  constexpr int STG = BM * 128 + 16384;
  constexpr int LPW = STG / 1024 / NWV;
  static_assert(LPW * 1024 * NWV == STG, "stage must split evenly over waves");
  extern __shared__ __attribute__((aligned(16))) char lds[];
  const int tid = threadIdx.x, wid = tid >> 6, lane = tid & 63, r32 = lane & 31, hi = lane >> 5;
  const int wm = wid / WN, wn = wid % WN;
  const int xcd = blockIdx.x & 7, loc = blockIdx.x >> 3;
  int tmi, tni, tsp = 0;
  if constexpr (MAP == 0) {
    tmi = xcd * (g.mt >> 3) + loc / g.nt; tni = loc % g.nt;
  } else if constexpr (MAP == 1) {
    tsp = xcd & 3; tmi = (xcd >> 2) * (g.mt >> 1) + loc / g.nt; tni = loc % g.nt;
  } else {
    tni = xcd * (g.nt >> 3) + loc % (g.nt >> 3); tmi = loc / (g.nt >> 3);
  }
  const int bm = tmi * BM, bn = tni * BN;
  const int kbase = tsp * g.K;
  const u16* Ab = g.A + (size_t)bm * g.lda + kbase;
  const u16* Bb = g.B + (size_t)kbase * g.ldb + bn;
  const u16* src[LPW];
#pragma unroll
  for (int i = 0; i < LPW; ++i) {
    const int p = (i * NWV + wid) * 1024 + lane * 16;
    if (p < BM * 128) {
      const int ci = p >> 4, row = ci >> 3, slot = ci & 7, cch = slot ^ ((row >> 1) & 7);
      src[i] = Ab + (size_t)row * g.lda + cch * 8;
    } else {
      const int q = p - BM * 128, st = q >> 9, rin = (q >> 6) & 7, cch = (q >> 4) & 3;
      const int kk = (st >> 2) * 8 + rin, c = (st & 3) * 32 + cch * 8;
      const int k = (kk & ~0xC) | ((kk & 4) << 1) | ((kk & 8) >> 1);
      src[i] = Bb + (size_t)k * g.ldb + c;
    }
  }
  auto stage = [&](int kt) {
    char* dst = lds + (kt % NS) * STG;
#pragma unroll
    for (int i = 0; i < LPW; ++i) {
      const int p = (i * NWV + wid) * 1024;
      const u16* sp = src[i] + ((p < BM * 128) ? (size_t)kt * BK : (size_t)kt * BK * g.ldb);
      __builtin_amdgcn_global_load_lds((const unsigned*)sp, (unsigned*)(dst + p), 16, 0, 0);
    }
  };
  f32x16 acc[TM][TN];
#pragma unroll
  for (int i = 0; i < TM; ++i)
#pragma unroll
    for (int j = 0; j < TN; ++j) acc[i][j] = f32x16{};

  const int nk = g.K / BK;
#pragma unroll
  for (int s = 0; s < NS - 1; ++s)
    if (s < nk) stage(s);
  const int vbB = (int)(uintptr_t)lds + BM * 128 + v_rd_base(lane) + wn * TN * 512;
  for (int kt = 0; kt < nk; ++kt) {
    const int rem = nk - 1 - kt;
    if (rem >= NS - 2) wait_vm<LPW*(NS - 2)>();
    else if (NS > 3 && rem == 1) wait_vm<LPW>();
    else wait_vm<0>();
    asm volatile("s_waitcnt lgkmcnt(0)" ::: "memory");
    __builtin_amdgcn_s_barrier();
    if (kt + NS - 1 < nk) stage(kt + NS - 1);
    const char* Ac = lds + (kt % NS) * STG;
    const int vb = vbB + (kt % NS) * STG;
    bf16x8 af[4][TM];
    s16x4 bl[4][TN], bh[4][TN];
#pragma unroll
    for (int ks = 0; ks < 4; ++ks) {
#pragma unroll
      for (int i = 0; i < TM; ++i)
        af[ks][i] = *reinterpret_cast<const bf16x8*>(Ac + a_off(wm * (BM / WM) + i * 32 + r32, (ks * 16 + hi * 8) * 2));
#pragma unroll
      for (int j = 0; j < TN; ++j) {
        if (ks == 0) b_frag<0>(vb, j, bl[ks][j], bh[ks][j]);
        else if (ks == 1) b_frag<1>(vb, j, bl[ks][j], bh[ks][j]);
        else if (ks == 2) b_frag<2>(vb, j, bl[ks][j], bh[ks][j]);
        else b_frag<3>(vb, j, bl[ks][j], bh[ks][j]);
      }
    }
    asm volatile("s_waitcnt lgkmcnt(0)" ::: "memory");
    SBAR();
#pragma unroll
    for (int ks = 0; ks < 4; ++ks)
#pragma unroll
      for (int j = 0; j < TN; ++j) {
        const bf16x8 bfr = {bl[ks][j][0], bl[ks][j][1], bl[ks][j][2], bl[ks][j][3], bh[ks][j][0], bh[ks][j][1], bh[ks][j][2], bh[ks][j][3]};
#pragma unroll
        for (int i = 0; i < TM; ++i) acc[i][j] = __builtin_amdgcn_mfma_f32_32x32x16_bf16(af[ks][i], bfr, acc[i][j], 0, 0, 0);
      }
  }
  __syncthreads();
  float* Ct = reinterpret_cast<float*>(lds);
#pragma unroll
  for (int i = 0; i < TM; ++i)
#pragma unroll
    for (int j = 0; j < TN; ++j)
#pragma unroll
      for (int r = 0; r < 16; ++r)
        Ct[(wm * (BM / WM) + i * 32 + crow(r, hi)) * 128 + wn * (BN / WN) + j * 32 + r32] = acc[i][j][r];
  __syncthreads();
  for (int idx = tid; idx < BM * 16; idx += 64 * NWV) {
    const int lr = idx >> 4, c8 = (idx & 15) * 8;
    const int row = bm + lr, col = bn + c8;
    const f32x4 v0 = *reinterpret_cast<const f32x4*>(Ct + lr * 128 + c8);
    const f32x4 v1 = *reinterpret_cast<const f32x4*>(Ct + lr * 128 + c8 + 4);
    float v[8] = {v0[0], v0[1], v0[2], v0[3], v1[0], v1[1], v1[2], v1[3]};
    if constexpr (EPI == EPI_F32PART) {
      f32x4* dst = reinterpret_cast<f32x4*>((float*)g.C + ((size_t)tsp * SEQ + row) * g.ldc + col);
      dst[0] = v0; dst[1] = v1;
    } else {
      bf16x8 o;
      if constexpr (EPI == EPI_BF16) {
#pragma unroll
        for (int e = 0; e < 8; ++e) o[e] = (short)f2bf(v[e]);
        *reinterpret_cast<bf16x8*>((u16*)g.C + (size_t)row * g.ldc + col) = o;
      } else if constexpr (EPI == EPI_GELU) {
#pragma unroll
        for (int e = 0; e < 8; ++e) {
          const float xb = rbf(v[e]);
          const float t = 0.7978845608028654f * (xb + 0.044715f * xb * xb * xb);
          const float th = 1.f - 2.f / (1.f + __expf(2.f * t));
          o[e] = (short)f2bf(0.5f * xb * (1.f + th));
        }
        if (col < FH) *reinterpret_cast<bf16x8*>((u16*)g.C + (size_t)row * FH + col) = o;
        else *reinterpret_cast<bf16x8*>((u16*)g.C2 + (size_t)row * FH + (col - FH)) = o;
      } else if constexpr (EPI == EPI_GATE) {
        const float bs = bf2f(g.aux0[row]);
        const bf16x8 z1 = *reinterpret_cast<const bf16x8*>(g.aux1 + (size_t)row * FH + col);
#pragma unroll
        for (int e = 0; e < 8; ++e) o[e] = (short)f2bf(bf2f((u16)z1[e]) * rbf(rbf(v[e]) + bs));
        *reinterpret_cast<bf16x8*>((u16*)g.C + (size_t)row * FH + col) = o;
      } else {
        const bf16x8 x1 = *reinterpret_cast<const bf16x8*>(g.aux0 + (size_t)row * DM + col);
#pragma unroll
        for (int e = 0; e < 8; ++e) o[e] = (short)f2bf(bf2f((u16)x1[e]) + rbf(g.mod[5 * DM + col + e] * rbf(v[e])));
        *reinterpret_cast<bf16x8*>((u16*)g.C + (size_t)row * DM + col) = o;
      }
    }
  }
}
template <int BM, int WM, int WN, int NS, int EPI, int MAP>
static void launch_gemm(GemmArgs g, int M, int N, int splits, hipStream_t stream) {
  constexpr int shm = NS * (BM * 128 + 16384);
  g.mt = M / BM; g.nt = N / 128;
  (void)hipFuncSetAttribute((const void*)k_gemm<BM, WM, WN, NS, EPI, MAP>, hipFuncAttributeMaxDynamicSharedMemorySize, shm);
  k_gemm<BM, WM, WN, NS, EPI, MAP><<<dim3(g.mt * g.nt * splits), dim3(64 * WM * WN), shm, stream>>>(g);
}

constexpr int SPLITA = 4;
__global__ __launch_bounds__(256) void k_reduce_ln(const float* __restrict__ part, const u16* __restrict__ x,
                                                   const float* __restrict__ mod, u16* __restrict__ x1,
                                                   u16* __restrict__ h2) {
  const int wid = threadIdx.x >> 6, lane = threadIdx.x & 63;
  const int row = blockIdx.x * 4 + wid;
  const bf16x8 raw = *reinterpret_cast<const bf16x8*>(x + (size_t)row * DM + lane * 8);
  float a[8];
#pragma unroll
  for (int i = 0; i < 8; ++i) a[i] = 0.f;
#pragma unroll
  for (int s = 0; s < SPLITA; ++s) {
    const f32x4* p = reinterpret_cast<const f32x4*>(part + ((size_t)s * SEQ + row) * DM + lane * 8);
    f32x4 p0 = p[0], p1 = p[1];
#pragma unroll
    for (int i = 0; i < 4; ++i) { a[i] += p0[i]; a[4 + i] += p1[i]; }
  }
  float v[8];
  float s = 0.f;
#pragma unroll
  for (int i = 0; i < 8; ++i) {
    const int cidx = lane * 8 + i;
    const float ga = mod[2 * DM + cidx];
    v[i] = rbf(bf2f((u16)raw[i]) + rbf(ga * rbf(a[i])));
    s += v[i];
  }
  s = wave_sum(s);
  const float mean = s * (1.f / DM);
  float q = 0.f;
#pragma unroll
  for (int i = 0; i < 8; ++i) { float d = v[i] - mean; q += d * d; }
  q = wave_sum(q);
  const float mb = rbf(mean), vb = rbf(q * (1.f / DM));
  const float den = rbf(sqrtf(rbf(vb + LN_EPS)));
  bf16x8 o, ox;
#pragma unroll
  for (int i = 0; i < 8; ++i) {
    const int cidx = lane * 8 + i;
    const float sb = mod[3 * DM + cidx], cb = mod[4 * DM + cidx];
    float n = rbf(rbf(v[i] - mb) / den);
    float h = rbf(rbf(n * rbf(1.f + cb)) + sb);
    o[i] = (short)f2bf(h);
    ox[i] = (short)f2bf(v[i]);
  }
  *reinterpret_cast<bf16x8*>(h2 + (size_t)row * DM + lane * 8) = o;
  *reinterpret_cast<bf16x8*>(x1 + (size_t)row * DM + lane * 8) = ox;
}

__global__ __launch_bounds__(256) void k_ln_z2(const u16* __restrict__ z2, u16* __restrict__ z2n) {
  const int wid = threadIdx.x >> 6, lane = threadIdx.x & 63;
  const int row = blockIdx.x * 4 + wid;
  const unsigned* p = reinterpret_cast<const unsigned*>(z2 + (size_t)row * FH);
  float v[14];
  float s = 0.f;
#pragma unroll
  for (int i = 0; i < 7; ++i) {
    unsigned w = p[i * 64 + lane];
    v[2 * i] = bf2f((u16)(w & 0xffff));
    v[2 * i + 1] = bf2f((u16)(w >> 16));
    s += v[2 * i] + v[2 * i + 1];
  }
  s = wave_sum(s);
  const float mean = s * (1.f / FH);
  float q = 0.f;
#pragma unroll
  for (int i = 0; i < 14; ++i) { float d = v[i] - mean; q += d * d; }
  q = wave_sum(q);
  const float mb = rbf(mean), vb = rbf(q * (1.f / FH));
  const float den = rbf(sqrtf(rbf(vb + LN_EPS)));
  unsigned* o = reinterpret_cast<unsigned*>(z2n + (size_t)row * FH);
#pragma unroll
  for (int i = 0; i < 7; ++i) {
    const u16 a = f2bf(rbf(v[2 * i] - mb) / den), b = f2bf(rbf(v[2 * i + 1] - mb) / den);
    o[i * 64 + lane] = (unsigned)a | ((unsigned)b << 16);
  }
}

constexpr int NW = 8, QBLK = 32, KVBLK = 64;
constexpr float SCALE = 0.088388347648318440f;
constexpr float THR = 8.f;
constexpr int LDQ = NH * HD, LDK = HD, LDO = NH * HD;
constexpr size_t SHM_V = KVBLK * HD * 2, SHM_K = KVBLK * HD * 2, SHM_ATTN = 8 * 16384 + NW * 64 * 4;
#define KSWZ(row, colB) ((row) * 256 + ((colB) ^ (((row) & 7) << 4)))

__device__ __forceinline__ void partialSM(f32x16& p0, f32x16& p1, float& m_reg, float& mn, float& alpha) {
  constexpr float C = SCALE * 1.4426950408889634f;
  float pmax = p0[0];
  for (int r = 1; r < 16; ++r) pmax = fmaxf(pmax, p0[r]);
  for (int r = 0; r < 16; ++r) pmax = fmaxf(pmax, p1[r]);
  { auto rr = __builtin_amdgcn_permlane32_swap(__float_as_uint(pmax), __float_as_uint(pmax), false, false);
    pmax = fmaxf(__uint_as_float(rr[0]), __uint_as_float(rr[1])); }
  if (__builtin_expect(__all(pmax - m_reg <= THR / SCALE), 1)) { mn = m_reg; alpha = 1.f; }
  else { mn = fmaxf(m_reg, pmax); alpha = __builtin_amdgcn_exp2f((m_reg - mn) * C); m_reg = mn; }
  float mnC = -mn * C;
  for (int r = 0; r < 16; ++r) p0[r] = fmaf(p0[r], C, mnC);
  for (int r = 0; r < 16; ++r) p1[r] = fmaf(p1[r], C, mnC);
  for (int r = 0; r < 16; ++r) p0[r] = __builtin_amdgcn_exp2f(p0[r]);
}
__device__ __forceinline__ void finishSM(f32x16& p0, f32x16& p1, float alpha, float& l_reg, bf16x8& pa0, bf16x8& pa1,
                                         bf16x8& pa2, bf16x8& pa3) {
  for (int r = 0; r < 16; ++r) p1[r] = __builtin_amdgcn_exp2f(p1[r]);
  float ps = 0;
  for (int r = 0; r < 16; ++r) ps += p0[r];
  for (int r = 0; r < 16; ++r) ps += p1[r];
  { auto rr = __builtin_amdgcn_permlane32_swap(__float_as_uint(ps), __float_as_uint(ps), false, false);
    ps = __uint_as_float(rr[0]) + __uint_as_float(rr[1]); }
  l_reg = l_reg * alpha + ps;
#define PK4(P, BASE, OUT) do { unsigned a0 = cvtpk(P[BASE + 0], P[BASE + 1]), a1 = cvtpk(P[BASE + 2], P[BASE + 3]);   \
    unsigned b0 = cvtpk(P[BASE + 4], P[BASE + 5]), b1 = cvtpk(P[BASE + 6], P[BASE + 7]);                              \
    auto r0 = __builtin_amdgcn_permlane32_swap(a0, b0, false, false); auto r1 = __builtin_amdgcn_permlane32_swap(a1, b1, false, false); \
    u32x4 w = {r0[0], r1[0], r0[1], r1[1]}; OUT = *reinterpret_cast<bf16x8*>(&w); } while (0)
  PK4(p0, 0, pa0); PK4(p0, 8, pa1); PK4(p1, 0, pa2); PK4(p1, 8, pa3);
#undef PK4
}
__device__ __forceinline__ void qkt(f32x16& p0, f32x16& p1, const char* Ks, const bf16x8* qr, int r32, int hi) {
  p0 = f32x16{}; p1 = f32x16{};
  for (int d0 = 0; d0 < 8; ++d0) { int cb = (d0 * 16 + hi * 8) * 2;
    bf16x8 b0 = *reinterpret_cast<const bf16x8*>(Ks + KSWZ(r32, cb));
    bf16x8 b1 = *reinterpret_cast<const bf16x8*>(Ks + KSWZ(32 + r32, cb));
    p0 = __builtin_amdgcn_mfma_f32_32x32x16_bf16(b0, qr[d0], p0, 0, 0, 0);
    p1 = __builtin_amdgcn_mfma_f32_32x32x16_bf16(b1, qr[d0], p1, 0, 0, 0); }
}
template <int D0>
__device__ __forceinline__ void pv_one(f32x16& od, int vb, bf16x8 pa0, bf16x8 pa1, bf16x8 pa2, bf16x8 pa3) {
  const s16x4 l0 = tr_read<v_rd_off(D0, 0, 0)>(vb), h0 = tr_read<v_rd_off(D0, 0, 1)>(vb), l1 = tr_read<v_rd_off(D0, 1, 0)>(vb), h1 = tr_read<v_rd_off(D0, 1, 1)>(vb);
  const s16x4 l2 = tr_read<v_rd_off(D0, 2, 0)>(vb), h2 = tr_read<v_rd_off(D0, 2, 1)>(vb), l3 = tr_read<v_rd_off(D0, 3, 0)>(vb), h3 = tr_read<v_rd_off(D0, 3, 1)>(vb);
  asm volatile("s_waitcnt lgkmcnt(0)" ::: "memory"); SBAR();
#define PK(L, H) (bf16x8){L[0], L[1], L[2], L[3], H[0], H[1], H[2], H[3]}
  od = __builtin_amdgcn_mfma_f32_32x32x16_bf16(pa0, PK(l0, h0), od, 0, 0, 0);
  od = __builtin_amdgcn_mfma_f32_32x32x16_bf16(pa1, PK(l1, h1), od, 0, 0, 0);
  od = __builtin_amdgcn_mfma_f32_32x32x16_bf16(pa2, PK(l2, h2), od, 0, 0, 0);
  od = __builtin_amdgcn_mfma_f32_32x32x16_bf16(pa3, PK(l3, h3), od, 0, 0, 0);
#undef PK
}
__device__ __forceinline__ void pv_d0(f32x16* o, int vb, bf16x8 pa0, bf16x8 pa1, bf16x8 pa2, bf16x8 pa3) {
  pv_one<0>(o[0], vb, pa0, pa1, pa2, pa3); pv_one<1>(o[1], vb, pa0, pa1, pa2, pa3);
  pv_one<2>(o[2], vb, pa0, pa1, pa2, pa3); pv_one<3>(o[3], vb, pa0, pa1, pa2, pa3);
}

template <int KS>
__device__ __forceinline__ void qproj_step(f32x16* qa, int vb, bf16x8 hb) {
  const s16x4 l0 = tr_read<v_rd_off(0, KS, 0)>(vb), h0 = tr_read<v_rd_off(0, KS, 1)>(vb);
  const s16x4 l1 = tr_read<v_rd_off(1, KS, 0)>(vb), h1 = tr_read<v_rd_off(1, KS, 1)>(vb);
  const s16x4 l2 = tr_read<v_rd_off(2, KS, 0)>(vb), h2 = tr_read<v_rd_off(2, KS, 1)>(vb);
  const s16x4 l3 = tr_read<v_rd_off(3, KS, 0)>(vb), h3 = tr_read<v_rd_off(3, KS, 1)>(vb);
  asm volatile("s_waitcnt lgkmcnt(0)" ::: "memory"); SBAR();
#define PK(L, H) (bf16x8){L[0], L[1], L[2], L[3], H[0], H[1], H[2], H[3]}
  qa[0] = __builtin_amdgcn_mfma_f32_32x32x16_bf16(PK(l0, h0), hb, qa[0], 0, 0, 0);
  qa[1] = __builtin_amdgcn_mfma_f32_32x32x16_bf16(PK(l1, h1), hb, qa[1], 0, 0, 0);
  qa[2] = __builtin_amdgcn_mfma_f32_32x32x16_bf16(PK(l2, h2), hb, qa[2], 0, 0, 0);
  qa[3] = __builtin_amdgcn_mfma_f32_32x32x16_bf16(PK(l3, h3), hb, qa[3], 0, 0, 0);
#undef PK
}

__global__ __launch_bounds__(64 * NW, 1) void k_attn(const u16* __restrict__ X, const float* __restrict__ mod,
                                                     const u16* __restrict__ Wq, const u16* __restrict__ Kc,
                                                     const u16* __restrict__ Vc, u16* __restrict__ O) {
  extern __shared__ __attribute__((aligned(16))) char lds[];
  const int bid = blockIdx.x;
  const int h = (bid & 7) + 8 * (bid >> 6);
  const long i0 = (long)((bid >> 3) & 7) * (QBLK * NW);
  const u16* Kh = Kc + (long)h * SK * HD;
  const u16* Vh = Vc + (long)h * SK * HD;
  u16* Ob = O + i0 * LDO + h * HD;
  const int tid = threadIdx.x, wid = tid >> 6, lane = tid & 63, r32 = lane & 31, hi = lane >> 5;
  char* V_lds = lds; char* K_lds = lds + 2 * SHM_V;
  float* ws = (float*)(lds + 8 * 16384) + wid * 64; float* li_l = ws; float* al_l = ws + 32;
  float m_reg = -1e30f, l_reg = 0; f32x16 o[4] = {}; bf16x8 qr[8];
  {
    const u16* xrow = X + (i0 + wid * QBLK + r32) * (long)DM + hi * 8;
    const u16* wqh = Wq + h * HD;
#pragma unroll
    for (int i = 0; i < 16; ++i) {
      const int pz = (i * NW + wid) * 1024 + lane * 16, t = pz >> 14, qq = pz & 16383;
      const int st = qq >> 9, rin = (qq >> 6) & 7, cch = (qq >> 4) & 3;
      const int kk = (st >> 2) * 8 + rin, c = (st & 3) * 32 + cch * 8;
      const int k = t * 64 + ((kk & ~0xC) | ((kk & 4) << 1) | ((kk & 8) >> 1));
      __builtin_amdgcn_global_load_lds((const unsigned*)(wqh + (long)k * (NH * HD) + c), (unsigned*)(lds + (i * NW + wid) * 1024), 16, 0, 0);
    }
    float s = 0.f;
#pragma unroll
    for (int g8 = 0; g8 < 4; ++g8) {
      bf16x8 t8[8];
#pragma unroll
      for (int i = 0; i < 8; ++i) t8[i] = *reinterpret_cast<const bf16x8*>(xrow + (g8 * 8 + i) * 16);
#pragma unroll
      for (int i = 0; i < 8; ++i)
#pragma unroll
        for (int e = 0; e < 8; ++e) s += bf2f((u16)t8[i][e]);
      SBAR();
    }
    s += __shfl_xor(s, 32, 64);
    const float mean = s * (1.f / DM);
    float q2 = 0.f;
#pragma unroll
    for (int g8 = 0; g8 < 4; ++g8) {
      bf16x8 t8[8];
#pragma unroll
      for (int i = 0; i < 8; ++i) t8[i] = *reinterpret_cast<const bf16x8*>(xrow + (g8 * 8 + i) * 16);
#pragma unroll
      for (int i = 0; i < 8; ++i)
#pragma unroll
        for (int e = 0; e < 8; ++e) { const float d = bf2f((u16)t8[i][e]) - mean; q2 += d * d; }
      SBAR();
    }
    q2 += __shfl_xor(q2, 32, 64);
    const float mb = rbf(mean), den = rbf(sqrtf(rbf(rbf(q2 * (1.f / DM)) + LN_EPS)));
    asm volatile("s_waitcnt vmcnt(0)" ::: "memory");
    __syncthreads();
    f32x16 qa[4] = {};
    const int vbq = (int)(uintptr_t)lds + v_rd_base(lane);
#pragma unroll
    for (int t = 0; t < 8; ++t) {
      bf16x8 hb[4];
#pragma unroll
      for (int i = 0; i < 4; ++i) {
        const int ks = t * 4 + i, c0 = ks * 16 + hi * 8;
        const bf16x8 xv = *reinterpret_cast<const bf16x8*>(xrow + ks * 16);
        const f32x4 sa0 = *reinterpret_cast<const f32x4*>(mod + c0), sa1 = *reinterpret_cast<const f32x4*>(mod + c0 + 4);
        const f32x4 ca0 = *reinterpret_cast<const f32x4*>(mod + DM + c0), ca1 = *reinterpret_cast<const f32x4*>(mod + DM + c0 + 4);
        float hv[8];
#pragma unroll
        for (int e = 0; e < 8; ++e) {
          const float sa = e < 4 ? sa0[e & 3] : sa1[e & 3], ca = e < 4 ? ca0[e & 3] : ca1[e & 3];
          const float n = rbf(rbf(bf2f((u16)xv[e]) - mb) / den);
          hv[e] = rbf(n * rbf(1.f + ca)) + sa;
        }
        u32x4 w = {cvtpk(hv[0], hv[1]), cvtpk(hv[2], hv[3]), cvtpk(hv[4], hv[5]), cvtpk(hv[6], hv[7])};
        hb[i] = *reinterpret_cast<bf16x8*>(&w);
      }
      qproj_step<0>(qa, vbq + t * 16384, hb[0]); qproj_step<1>(qa, vbq + t * 16384, hb[1]);
      qproj_step<2>(qa, vbq + t * 16384, hb[2]); qproj_step<3>(qa, vbq + t * 16384, hb[3]);
      SBAR();
    }
    __syncthreads();
#define PK4Q(P, BASE, OUT) do { unsigned a0 = cvtpk(P[BASE + 0], P[BASE + 1]), a1 = cvtpk(P[BASE + 2], P[BASE + 3]);   \
    unsigned b0 = cvtpk(P[BASE + 4], P[BASE + 5]), b1 = cvtpk(P[BASE + 6], P[BASE + 7]);                              \
    auto r0 = __builtin_amdgcn_permlane32_swap(a0, b0, false, false); auto r1 = __builtin_amdgcn_permlane32_swap(a1, b1, false, false); \
    u32x4 w = {r0[0], r1[0], r0[1], r1[1]}; OUT = *reinterpret_cast<bf16x8*>(&w); } while (0)
    PK4Q(qa[0], 0, qr[0]); PK4Q(qa[0], 8, qr[1]); PK4Q(qa[1], 0, qr[2]); PK4Q(qa[1], 8, qr[3]);
    PK4Q(qa[2], 0, qr[4]); PK4Q(qa[2], 8, qr[5]); PK4Q(qa[3], 0, qr[6]); PK4Q(qa[3], 8, qr[7]);
#undef PK4Q
  }
  const int sr = tid >> 4, sc = (tid & 15) * 8, vst0 = v_st(sr, sc), vst1 = v_st(32 + sr, sc);
  const int vb0 = (int)(uintptr_t)V_lds + v_rd_base(lane);
  struct { bf16x8 vs0, vs1, ks0, ks1; } sr_[2];
#define LD8(p) (*reinterpret_cast<const bf16x8*>(p))
#define SLOAD(i, k0) do { sr_[i].vs0 = LD8(&Vh[(long)((k0) + sr) * LDK + sc]); sr_[i].vs1 = LD8(&Vh[(long)((k0) + 32 + sr) * LDK + sc]); \
    sr_[i].ks0 = LD8(&Kh[(long)((k0) + sr) * LDK + sc]); sr_[i].ks1 = LD8(&Kh[(long)((k0) + 32 + sr) * LDK + sc]); } while (0)
#define SWRITE(b, i) do { *(bf16x8*)(V_lds + (b) * SHM_V + vst0) = sr_[i].vs0;          \
    *(bf16x8*)(V_lds + (b) * SHM_V + vst1) = sr_[i].vs1; int kc = sc * 2;               \
    *(bf16x8*)(K_lds + (b) * SHM_K + KSWZ(sr, kc)) = sr_[i].ks0;                       \
    *(bf16x8*)(K_lds + (b) * SHM_K + KSWZ(32 + sr, kc)) = sr_[i].ks1; } while (0)
#define SWAIT() asm volatile("s_waitcnt vmcnt(4)" ::: "memory")
#define RESC(a) do { if (__any((a) < 1.f)) { if (hi == 0) al_l[r32] = (a); asm volatile("s_waitcnt lgkmcnt(0)" ::: "memory"); \
    for (int d = 0; d < 4; ++d) for (int r = 0; r < 16; ++r) o[d][r] *= al_l[crow(r, hi)]; } } while (0)
  f32x16 pA0, pA1, pB0, pB1; float mnA, mnB, alA, alB; bf16x8 pa0, pa1, pa2, pa3; const int NT = SK / KVBLK;
  constexpr int SE = 0, SO = 1;
  SLOAD(SE, 0); asm volatile("s_waitcnt vmcnt(0)" ::: "memory"); SWRITE(0, SE); __syncthreads();
  qkt(pA0, pA1, K_lds, qr, r32, hi); partialSM(pA0, pA1, m_reg, mnA, alA);
  SLOAD(SO, KVBLK); SLOAD(SE, 2 * KVBLK);
  SWAIT(); SWRITE(1, SO); __syncthreads();
  for (int j = 1; j + 1 < NT; j += 2) {
    SBAR(); qkt(pB0, pB1, K_lds + SHM_K, qr, r32, hi);
    finishSM(pA0, pA1, alA, l_reg, pa0, pa1, pa2, pa3); SBAR();
    SLOAD(SO, (j + 2) * KVBLK); SBAR();
    pv_d0(o, vb0, pa0, pa1, pa2, pa3); partialSM(pB0, pB1, m_reg, mnB, alB);
    __syncthreads(); SWAIT(); SWRITE(0, SE);
    RESC(alB); __syncthreads();
    SBAR(); qkt(pA0, pA1, K_lds, qr, r32, hi);
    finishSM(pB0, pB1, alB, l_reg, pa0, pa1, pa2, pa3); SBAR();
    if (j + 3 < NT) SLOAD(SE, (j + 3) * KVBLK); SBAR();
    pv_d0(o, vb0 + (int)SHM_V, pa0, pa1, pa2, pa3); partialSM(pA0, pA1, m_reg, mnA, alA);
    __syncthreads(); SWAIT(); SWRITE(1, SO);
    RESC(alA); __syncthreads();
  }
  SBAR(); qkt(pB0, pB1, K_lds + SHM_K, qr, r32, hi);
  finishSM(pA0, pA1, alA, l_reg, pa0, pa1, pa2, pa3); SBAR();
  pv_d0(o, vb0, pa0, pa1, pa2, pa3); partialSM(pB0, pB1, m_reg, mnB, alB);
  __syncthreads(); RESC(alB);
  finishSM(pB0, pB1, alB, l_reg, pa0, pa1, pa2, pa3); SBAR();
  pv_d0(o, vb0 + (int)SHM_V, pa0, pa1, pa2, pa3);
  if (hi == 0) li_l[r32] = l_reg;
  asm volatile("s_waitcnt lgkmcnt(0)" ::: "memory");
  float rli[16];
#pragma unroll
  for (int r = 0; r < 16; ++r) rli[r] = __builtin_amdgcn_rcpf(li_l[crow(r, hi)]);
  u16* Ow = Ob + (long)(wid * QBLK) * LDO;
#pragma unroll
  for (int r = 0; r < 16; ++r) { int orow = crow(r, hi);
    for (int d0 = 0; d0 < 4; ++d0) Ow[(long)orow * LDO + d0 * 32 + r32] = f2bf(o[d0][r] * rli[r]); }
#undef SLOAD
#undef SWRITE
#undef SWAIT
#undef RESC
#undef LD8
}

extern "C" void kernel_launch(void* const* d_in, const int* in_sizes, int n_in, void* d_out, int out_size, void* d_ws,
                              size_t ws_size, hipStream_t stream) {
  const u16* x = (const u16*)d_in[0];
  const u16* kc = (const u16*)d_in[1];
  const u16* vc = (const u16*)d_in[2];
  const u16* wq = (const u16*)d_in[3];
  const u16* wo = (const u16*)d_in[4];
  const u16* w_in = (const u16*)d_in[6];
  const u16* w_sp = (const u16*)d_in[7];
  const u16* b_sp = (const u16*)d_in[8];
  const u16* w_out = (const u16*)d_in[9];
  const u16* cvec = (const u16*)d_in[10];
  const u16* w_mod = (const u16*)d_in[11];
  u16* out = (u16*)d_out;

  char* ws = (char*)d_ws;
  size_t off = 0;
  auto alloc = [&](size_t bytes) { char* p = ws + off; off += (bytes + 255) & ~(size_t)255; return p; };
  float* mod = (float*)alloc(6 * DM * 4);
  u16* h1 = (u16*)alloc((size_t)SEQ * DM * 2);
  u16* q = (u16*)alloc((size_t)SEQ * NH * HD * 2);
  u16* o = (u16*)alloc((size_t)SEQ * NH * HD * 2);
  float* part = (float*)alloc((size_t)SPLITA * SEQ * DM * 4);
  u16* x1 = (u16*)alloc((size_t)SEQ * DM * 2);
  u16* h2 = (u16*)alloc((size_t)SEQ * DM * 2);
  u16* z1 = (u16*)alloc((size_t)SEQ * FH * 2);
  u16* z2 = (u16*)alloc((size_t)SEQ * FH * 2);
  u16* z2n = (u16*)alloc((size_t)SEQ * FH * 2);
  u16* u = (u16*)alloc((size_t)SEQ * FH * 2);

  (void)hipFuncSetAttribute((const void*)k_attn, hipFuncAttributeMaxDynamicSharedMemorySize, (int)SHM_ATTN);

  k_mod<<<dim3(6 * DM / 64), dim3(256), 0, stream>>>(cvec, w_mod, mod);
  k_attn<<<dim3(256), dim3(512), SHM_ATTN, stream>>>(x, mod, wq, kc, vc, o);
  {
    GemmArgs g{o, wo, part, nullptr, nullptr, nullptr, nullptr, NH * HD, DM, DM, NH * HD / SPLITA};
    launch_gemm<128, 2, 4, 4, EPI_F32PART, 1>(g, SEQ, DM, SPLITA, stream);
  }
  k_reduce_ln<<<dim3(SEQ / 4), dim3(256), 0, stream>>>(part, x, mod, x1, h2);
  {
    GemmArgs g{h2, w_in, z1, z2, nullptr, nullptr, nullptr, DM, FF, FH, DM};
    launch_gemm<128, 2, 4, 4, EPI_GELU, 0>(g, SEQ, FF, 1, stream);
  }
  k_ln_z2<<<dim3(SEQ / 4), dim3(256), 0, stream>>>(z2, z2n);
  {
    GemmArgs g{w_sp, z2n, u, nullptr, b_sp, z1, nullptr, SEQ, FH, FH, SEQ};
    launch_gemm<64, 2, 4, 5, EPI_GATE, 0>(g, SEQ, FH, 1, stream);
  }
  {
    GemmArgs g{u, w_out, out, nullptr, x1, nullptr, mod, FH, DM, DM, FH};
    launch_gemm<32, 1, 4, 4, EPI_FINAL, 0>(g, SEQ, DM, 1, stream);
  }
}
```

```cpp
#include <hip/hip_runtime.h>
#include <hip/hip_bf16.h>
#include <stdint.h>

typedef unsigned short u16;
typedef short bf16x8 __attribute__((ext_vector_type(8)));
typedef short s16x4 __attribute__((ext_vector_type(4)));
typedef float f32x16 __attribute__((ext_vector_type(16)));
typedef float f32x4 __attribute__((ext_vector_type(4)));
typedef unsigned u32x4 __attribute__((ext_vector_type(4)));

constexpr int SEQ = 2048, DM = 512, NH = 32, HD = 128, SK = 2048, FH = 896, FF = 1792;
constexpr float LN_EPS = 1e-5f;

__device__ __forceinline__ u16 f2bf(float f) {
  unsigned u = __float_as_uint(f);
  u += 0x7fffu + ((u >> 16) & 1u);
  return (u16)(u >> 16);
}
__device__ __forceinline__ float bf2f(u16 b) { return __uint_as_float(((unsigned)b) << 16); }
__device__ __forceinline__ float rbf(float f) { return bf2f(f2bf(f)); }
__device__ __forceinline__ int crow(int r, int hi) { return (r & 3) + 8 * (r >> 2) + 4 * hi; }
__device__ __forceinline__ unsigned cvtpk(float lo, float hi) {
  unsigned r;
  asm volatile("v_cvt_pk_bf16_f32 %0, %1, %2" : "=v"(r) : "v"(lo), "v"(hi));
  return r;
}
#define SBAR() __builtin_amdgcn_sched_barrier(0)

__device__ __forceinline__ float wave_sum(float v) {
#pragma unroll
  for (int o = 32; o >= 1; o >>= 1) v += __shfl_xor(v, o, 64);
  return v;
}
__global__ __launch_bounds__(256) void k_mod(const u16* __restrict__ c, const u16* __restrict__ wmod,
                                             float* __restrict__ mod, const u16* __restrict__ x, u16* __restrict__ n1) {
  __shared__ float red[32][65];
  __shared__ float sc[DM];
  const int tid = threadIdx.x;
  if (blockIdx.x >= 6 * DM / 64) {
    const int row = (blockIdx.x - 6 * DM / 64) * 4 + (tid >> 6), lane = tid & 63;
    const bf16x8 raw = *reinterpret_cast<const bf16x8*>(x + (size_t)row * DM + lane * 8);
    float v[8], s = 0.f;
#pragma unroll
    for (int i = 0; i < 8; ++i) { v[i] = bf2f((u16)raw[i]); s += v[i]; }
    s = wave_sum(s);
    const float mean = s * (1.f / DM);
    float q = 0.f;
#pragma unroll
    for (int i = 0; i < 8; ++i) { float d = v[i] - mean; q += d * d; }
    q = wave_sum(q);
    const float mb = rbf(mean), den = rbf(sqrtf(rbf(rbf(q * (1.f / DM)) + LN_EPS)));
    bf16x8 o;
#pragma unroll
    for (int i = 0; i < 8; ++i) o[i] = (short)f2bf(rbf(v[i] - mb) / den);
    *reinterpret_cast<bf16x8*>(n1 + (size_t)row * DM + lane * 8) = o;
    return;
  }
  for (int i = tid; i < DM; i += 256) {
    float x = bf2f(c[i]);
    sc[i] = rbf(x / (1.f + __expf(-x)));
  }
  __syncthreads();
  const int cc = tid & 7, kg = tid >> 3;
  const int j0 = blockIdx.x * 64 + cc * 8;
  bf16x8 w[16];
#pragma unroll
  for (int i = 0; i < 16; ++i) w[i] = *reinterpret_cast<const bf16x8*>(wmod + (size_t)(kg * 16 + i) * (6 * DM) + j0);
  float acc[8];
#pragma unroll
  for (int e = 0; e < 8; ++e) acc[e] = 0.f;
#pragma unroll
  for (int i = 0; i < 16; ++i) {
    const float sv = sc[kg * 16 + i];
#pragma unroll
    for (int e = 0; e < 8; ++e) acc[e] += sv * bf2f((u16)w[i][e]);
  }
#pragma unroll
  for (int e = 0; e < 8; ++e) red[kg][cc * 8 + e] = acc[e];
  __syncthreads();
  if (tid < 64) {
    float t = 0.f;
#pragma unroll
    for (int g = 0; g < 32; ++g) t += red[g][tid];
    mod[blockIdx.x * 64 + tid] = rbf(t);
  }
}

__global__ __launch_bounds__(256) void k_ln_mod(const u16* __restrict__ x, const float* __restrict__ mod,
                                                u16* __restrict__ h1) {
  const int wid = threadIdx.x >> 6, lane = threadIdx.x & 63;
  const int row = blockIdx.x * 4 + wid;
  const bf16x8 raw = *reinterpret_cast<const bf16x8*>(x + (size_t)row * DM + lane * 8);
  float v[8];
  float s = 0.f;
#pragma unroll
  for (int i = 0; i < 8; ++i) { v[i] = bf2f((u16)raw[i]); s += v[i]; }
  s = wave_sum(s);
  const float mean = s * (1.f / DM);
  float q = 0.f;
#pragma unroll
  for (int i = 0; i < 8; ++i) { float d = v[i] - mean; q += d * d; }
  q = wave_sum(q);
  const float mb = rbf(mean), vb = rbf(q * (1.f / DM));
  const float den = rbf(sqrtf(rbf(vb + LN_EPS)));
  bf16x8 o;
#pragma unroll
  for (int i = 0; i < 8; ++i) {
    const int cidx = lane * 8 + i;
    const float sa = mod[cidx], ca = mod[DM + cidx];
    float n = rbf(rbf(v[i] - mb) / den);
    float h = rbf(rbf(n * rbf(1.f + ca)) + sa);
    o[i] = (short)f2bf(h);
  }
  *reinterpret_cast<bf16x8*>(h1 + (size_t)row * DM + lane * 8) = o;
}

__device__ __forceinline__ int a_off(int row, int colB) { return row * 128 + (colB ^ (((row >> 1) & 7) << 4)); }
__device__ __forceinline__ int v_st(int k, int c) {
  const int kk = (k & ~0xC) | ((k & 4) << 1) | ((k & 8) >> 1);
  return ((kk >> 3) * 4 + (c >> 5)) * 512 + ((kk & 7) * 32 + (c & 31)) * 2;
}
__device__ __forceinline__ int v_rd_base(int lane) {
  return ((lane & 3) << 3) | (((lane >> 2) & 3) << 6) | (((lane >> 4) & 1) << 5) | (((lane >> 5) & 1) << 8);
}
constexpr int v_rd_off(int d0, int ks, int half) { return d0 * 512 + ks * 4096 + half * 2048; }
template <int OFF>
__device__ __forceinline__ s16x4 tr_read(int vb) {
  s16x4 r;
  asm volatile("ds_read_b64_tr_b16 %0, %1 offset:%2" : "=&v"(r) : "v"(vb), "i"(OFF) : "memory");
  return r;
}

enum { EPI_BF16 = 0, EPI_F32PART = 1, EPI_GELU = 2, EPI_GATE = 3, EPI_FINAL = 4 };

struct GemmArgs {
  const u16* A; const u16* B;
  void* C; void* C2;
  const u16* aux0; const u16* aux1; const float* mod;
  int lda, ldb, ldc, K;
  int mt, nt;
};

template <int OFFK>
__device__ __forceinline__ void b_frag(int vb, int j, s16x4& lo, s16x4& hi) {
  if (j == 0) { lo = tr_read<v_rd_off(0, OFFK, 0)>(vb); hi = tr_read<v_rd_off(0, OFFK, 1)>(vb); }
  else if (j == 1) { lo = tr_read<v_rd_off(1, OFFK, 0)>(vb); hi = tr_read<v_rd_off(1, OFFK, 1)>(vb); }
  else if (j == 2) { lo = tr_read<v_rd_off(2, OFFK, 0)>(vb); hi = tr_read<v_rd_off(2, OFFK, 1)>(vb); }
  else { lo = tr_read<v_rd_off(3, OFFK, 0)>(vb); hi = tr_read<v_rd_off(3, OFFK, 1)>(vb); }
}
template <int N> __device__ __forceinline__ void wait_vm() {
  if constexpr (N == 0) asm volatile("s_waitcnt vmcnt(0)" ::: "memory");
  else if constexpr (N == 1) asm volatile("s_waitcnt vmcnt(1)" ::: "memory");
  else if constexpr (N == 2) asm volatile("s_waitcnt vmcnt(2)" ::: "memory");
  else if constexpr (N == 3) asm volatile("s_waitcnt vmcnt(3)" ::: "memory");
  else if constexpr (N == 4) asm volatile("s_waitcnt vmcnt(4)" ::: "memory");
  else if constexpr (N == 5) asm volatile("s_waitcnt vmcnt(5)" ::: "memory");
  else if constexpr (N == 6) asm volatile("s_waitcnt vmcnt(6)" ::: "memory");
  else if constexpr (N == 8) asm volatile("s_waitcnt vmcnt(8)" ::: "memory");
  else if constexpr (N == 9) asm volatile("s_waitcnt vmcnt(9)" ::: "memory");
  else if constexpr (N == 10) asm volatile("s_waitcnt vmcnt(10)" ::: "memory");
  else if constexpr (N == 12) asm volatile("s_waitcnt vmcnt(12)" ::: "memory");
  else if constexpr (N == 15) asm volatile("s_waitcnt vmcnt(15)" ::: "memory");
  else static_assert(N < 0, "unsupported vmcnt");
}

template <int BM, int WM, int WN, int NS, int EPI, int MAP>
__global__ __launch_bounds__(64 * WM * WN) void k_gemm(GemmArgs g) {
  constexpr int BN = 128, BK = 64, NWV = WM * WN;
  constexpr int TM = BM / WM / 32, TN = BN / WN / 32;
  constexpr int STG = BM * 128 + 16384;
  constexpr int LPW = STG / 1024 / NWV;
  static_assert(LPW * 1024 * NWV == STG, "stage must split evenly over waves");
  extern __shared__ __attribute__((aligned(16))) char lds[];
  const int tid = threadIdx.x, wid = tid >> 6, lane = tid & 63, r32 = lane & 31, hi = lane >> 5;
  const int wm = wid / WN, wn = wid % WN;
  const int xcd = blockIdx.x & 7, loc = blockIdx.x >> 3;
  int tmi, tni, tsp = 0;
  if constexpr (MAP == 0) {
    tmi = xcd * (g.mt >> 3) + loc / g.nt; tni = loc % g.nt;
  } else if constexpr (MAP == 1) {
    tsp = xcd & 1; tmi = (xcd >> 1) * (g.mt >> 2) + loc / g.nt; tni = loc % g.nt;
  } else {
    tni = xcd * (g.nt >> 3) + loc % (g.nt >> 3); tmi = loc / (g.nt >> 3);
  }
  const int bm = tmi * BM, bn = tni * BN;
  const int kbase = tsp * g.K;
  const u16* Ab = g.A + (size_t)bm * g.lda + kbase;
  const u16* Bb = g.B + (size_t)kbase * g.ldb + bn;
  const u16* src[LPW];
#pragma unroll
  for (int i = 0; i < LPW; ++i) {
    const int p = (i * NWV + wid) * 1024 + lane * 16;
    if (p < BM * 128) {
      const int ci = p >> 4, row = ci >> 3, slot = ci & 7, cch = slot ^ ((row >> 1) & 7);
      src[i] = Ab + (size_t)row * g.lda + cch * 8;
    } else {
      const int q = p - BM * 128, st = q >> 9, rin = (q >> 6) & 7, cch = (q >> 4) & 3;
      const int kk = (st >> 2) * 8 + rin, c = (st & 3) * 32 + cch * 8;
      const int k = (kk & ~0xC) | ((kk & 4) << 1) | ((kk & 8) >> 1);
      src[i] = Bb + (size_t)k * g.ldb + c;
    }
  }
  auto stage = [&](int kt) {
    char* dst = lds + (kt % NS) * STG;
#pragma unroll
    for (int i = 0; i < LPW; ++i) {
      const int p = (i * NWV + wid) * 1024;
      const u16* sp = src[i] + ((p < BM * 128) ? (size_t)kt * BK : (size_t)kt * BK * g.ldb);
      __builtin_amdgcn_global_load_lds((const unsigned*)sp, (unsigned*)(dst + p), 16, 0, 0);
    }
  };
  f32x16 acc[TM][TN];
#pragma unroll
  for (int i = 0; i < TM; ++i)
#pragma unroll
    for (int j = 0; j < TN; ++j) acc[i][j] = f32x16{};

  const int nk = g.K / BK;
#pragma unroll
  for (int s = 0; s < NS - 1; ++s)
    if (s < nk) stage(s);
  const int vbB = (int)(uintptr_t)lds + BM * 128 + v_rd_base(lane) + wn * TN * 512;
  for (int kt = 0; kt < nk; ++kt) {
    const int rem = nk - 1 - kt;
    if (rem >= NS - 2) wait_vm<LPW*(NS - 2)>();
    else if (NS > 3 && rem == 1) wait_vm<LPW>();
    else wait_vm<0>();
    asm volatile("s_waitcnt lgkmcnt(0)" ::: "memory");
    __builtin_amdgcn_s_barrier();
    if (kt + NS - 1 < nk) stage(kt + NS - 1);
    const char* Ac = lds + (kt % NS) * STG;
    const int vb = vbB + (kt % NS) * STG;
    bf16x8 af[4][TM];
    s16x4 bl[4][TN], bh[4][TN];
#pragma unroll
    for (int ks = 0; ks < 4; ++ks) {
#pragma unroll
      for (int i = 0; i < TM; ++i)
        af[ks][i] = *reinterpret_cast<const bf16x8*>(Ac + a_off(wm * (BM / WM) + i * 32 + r32, (ks * 16 + hi * 8) * 2));
#pragma unroll
      for (int j = 0; j < TN; ++j) {
        if (ks == 0) b_frag<0>(vb, j, bl[ks][j], bh[ks][j]);
        else if (ks == 1) b_frag<1>(vb, j, bl[ks][j], bh[ks][j]);
        else if (ks == 2) b_frag<2>(vb, j, bl[ks][j], bh[ks][j]);
        else b_frag<3>(vb, j, bl[ks][j], bh[ks][j]);
      }
    }
    asm volatile("s_waitcnt lgkmcnt(0)" ::: "memory");
    SBAR();
#pragma unroll
    for (int ks = 0; ks < 4; ++ks)
#pragma unroll
      for (int j = 0; j < TN; ++j) {
        const bf16x8 bfr = {bl[ks][j][0], bl[ks][j][1], bl[ks][j][2], bl[ks][j][3], bh[ks][j][0], bh[ks][j][1], bh[ks][j][2], bh[ks][j][3]};
#pragma unroll
        for (int i = 0; i < TM; ++i) acc[i][j] = __builtin_amdgcn_mfma_f32_32x32x16_bf16(af[ks][i], bfr, acc[i][j], 0, 0, 0);
      }
  }
  __syncthreads();
  float* Ct = reinterpret_cast<float*>(lds);
#pragma unroll
  for (int i = 0; i < TM; ++i)
#pragma unroll
    for (int j = 0; j < TN; ++j)
#pragma unroll
      for (int r = 0; r < 16; ++r)
        Ct[(wm * (BM / WM) + i * 32 + crow(r, hi)) * 128 + wn * (BN / WN) + j * 32 + r32] = acc[i][j][r];
  __syncthreads();
  for (int idx = tid; idx < BM * 16; idx += 64 * NWV) {
    const int lr = idx >> 4, c8 = (idx & 15) * 8;
    const int row = bm + lr, col = bn + c8;
    const f32x4 v0 = *reinterpret_cast<const f32x4*>(Ct + lr * 128 + c8);
    const f32x4 v1 = *reinterpret_cast<const f32x4*>(Ct + lr * 128 + c8 + 4);
    float v[8] = {v0[0], v0[1], v0[2], v0[3], v1[0], v1[1], v1[2], v1[3]};
    if constexpr (EPI == EPI_F32PART) {
      f32x4* dst = reinterpret_cast<f32x4*>((float*)g.C + ((size_t)tsp * SEQ + row) * g.ldc + col);
      dst[0] = v0; dst[1] = v1;
    } else {
      bf16x8 o;
      if constexpr (EPI == EPI_BF16) {
#pragma unroll
        for (int e = 0; e < 8; ++e) o[e] = (short)f2bf(v[e]);
        *reinterpret_cast<bf16x8*>((u16*)g.C + (size_t)row * g.ldc + col) = o;
      } else if constexpr (EPI == EPI_GELU) {
#pragma unroll
        for (int e = 0; e < 8; ++e) {
          const float xb = rbf(v[e]);
          const float t = 0.7978845608028654f * (xb + 0.044715f * xb * xb * xb);
          const float th = 1.f - 2.f / (1.f + __expf(2.f * t));
          o[e] = (short)f2bf(0.5f * xb * (1.f + th));
        }
        if (col < FH) *reinterpret_cast<bf16x8*>((u16*)g.C + (size_t)row * FH + col) = o;
        else *reinterpret_cast<bf16x8*>((u16*)g.C2 + (size_t)row * FH + (col - FH)) = o;
      } else if constexpr (EPI == EPI_GATE) {
        const float bs = bf2f(g.aux0[row]);
        const bf16x8 z1 = *reinterpret_cast<const bf16x8*>(g.aux1 + (size_t)row * FH + col);
#pragma unroll
        for (int e = 0; e < 8; ++e) o[e] = (short)f2bf(bf2f((u16)z1[e]) * rbf(rbf(v[e]) + bs));
        *reinterpret_cast<bf16x8*>((u16*)g.C + (size_t)row * FH + col) = o;
      } else {
        const bf16x8 x1 = *reinterpret_cast<const bf16x8*>(g.aux0 + (size_t)row * DM + col);
#pragma unroll
        for (int e = 0; e < 8; ++e) o[e] = (short)f2bf(bf2f((u16)x1[e]) + rbf(g.mod[5 * DM + col + e] * rbf(v[e])));
        *reinterpret_cast<bf16x8*>((u16*)g.C + (size_t)row * DM + col) = o;
      }
    }
  }
}
template <int BM, int WM, int WN, int NS, int EPI, int MAP>
static void launch_gemm(GemmArgs g, int M, int N, int splits, hipStream_t stream) {
  constexpr int shm = NS * (BM * 128 + 16384);
  g.mt = M / BM; g.nt = N / 128;
  (void)hipFuncSetAttribute((const void*)k_gemm<BM, WM, WN, NS, EPI, MAP>, hipFuncAttributeMaxDynamicSharedMemorySize, shm);
  k_gemm<BM, WM, WN, NS, EPI, MAP><<<dim3(g.mt * g.nt * splits), dim3(64 * WM * WN), shm, stream>>>(g);
}

constexpr int SPLITA = 2;
__global__ __launch_bounds__(256) void k_reduce_ln(const float* __restrict__ part, const u16* __restrict__ x,
                                                   const float* __restrict__ mod, u16* __restrict__ x1,
                                                   u16* __restrict__ h2) {
  const int wid = threadIdx.x >> 6, lane = threadIdx.x & 63;
  const int row = blockIdx.x * 4 + wid;
  const bf16x8 raw = *reinterpret_cast<const bf16x8*>(x + (size_t)row * DM + lane * 8);
  float a[8];
#pragma unroll
  for (int i = 0; i < 8; ++i) a[i] = 0.f;
#pragma unroll
  for (int s = 0; s < SPLITA; ++s) {
    const f32x4* p = reinterpret_cast<const f32x4*>(part + ((size_t)s * SEQ + row) * DM + lane * 8);
    f32x4 p0 = p[0], p1 = p[1];
#pragma unroll
    for (int i = 0; i < 4; ++i) { a[i] += p0[i]; a[4 + i] += p1[i]; }
  }
  float v[8];
  float s = 0.f;
#pragma unroll
  for (int i = 0; i < 8; ++i) {
    const int cidx = lane * 8 + i;
    const float ga = mod[2 * DM + cidx];
    v[i] = rbf(bf2f((u16)raw[i]) + rbf(ga * rbf(a[i])));
    s += v[i];
  }
  s = wave_sum(s);
  const float mean = s * (1.f / DM);
  float q = 0.f;
#pragma unroll
  for (int i = 0; i < 8; ++i) { float d = v[i] - mean; q += d * d; }
  q = wave_sum(q);
  const float mb = rbf(mean), vb = rbf(q * (1.f / DM));
  const float den = rbf(sqrtf(rbf(vb + LN_EPS)));
  bf16x8 o, ox;
#pragma unroll
  for (int i = 0; i < 8; ++i) {
    const int cidx = lane * 8 + i;
    const float sb = mod[3 * DM + cidx], cb = mod[4 * DM + cidx];
    float n = rbf(rbf(v[i] - mb) / den);
    float h = rbf(rbf(n * rbf(1.f + cb)) + sb);
    o[i] = (short)f2bf(h);
    ox[i] = (short)f2bf(v[i]);
  }
  *reinterpret_cast<bf16x8*>(h2 + (size_t)row * DM + lane * 8) = o;
  *reinterpret_cast<bf16x8*>(x1 + (size_t)row * DM + lane * 8) = ox;
}

__global__ __launch_bounds__(256) void k_ln_z2(const u16* __restrict__ z2, u16* __restrict__ z2n) {
  const int wid = threadIdx.x >> 6, lane = threadIdx.x & 63;
  const int row = blockIdx.x * 4 + wid;
  const unsigned* p = reinterpret_cast<const unsigned*>(z2 + (size_t)row * FH);
  float v[14];
  float s = 0.f;
#pragma unroll
  for (int i = 0; i < 7; ++i) {
    unsigned w = p[i * 64 + lane];
    v[2 * i] = bf2f((u16)(w & 0xffff));
    v[2 * i + 1] = bf2f((u16)(w >> 16));
    s += v[2 * i] + v[2 * i + 1];
  }
  s = wave_sum(s);
  const float mean = s * (1.f / FH);
  float q = 0.f;
#pragma unroll
  for (int i = 0; i < 14; ++i) { float d = v[i] - mean; q += d * d; }
  q = wave_sum(q);
  const float mb = rbf(mean), vb = rbf(q * (1.f / FH));
  const float den = rbf(sqrtf(rbf(vb + LN_EPS)));
  unsigned* o = reinterpret_cast<unsigned*>(z2n + (size_t)row * FH);
#pragma unroll
  for (int i = 0; i < 7; ++i) {
    const u16 a = f2bf(rbf(v[2 * i] - mb) / den), b = f2bf(rbf(v[2 * i + 1] - mb) / den);
    o[i * 64 + lane] = (unsigned)a | ((unsigned)b << 16);
  }
}

constexpr int NW = 8, QBLK = 32, KVBLK = 64;
constexpr float SCALE = 0.088388347648318440f;
constexpr float THR = 0.f;
constexpr int LDQ = NH * HD, LDK = HD, LDO = NH * HD;
constexpr size_t SHM_V = KVBLK * HD * 2, SHM_K = KVBLK * HD * 2, SHM_ATTN = 8 * 16384 + NW * 64 * 4 + 4096;
#define KSWZ(row, colB) ((row) * 256 + ((colB) ^ (((row) & 7) << 4)))

__device__ __forceinline__ void partialSM(f32x16& p0, f32x16& p1, float& m_reg, float& mn, float& alpha) {
  constexpr float C = SCALE * 1.4426950408889634f;
  float pmax = p0[0];
  for (int r = 1; r < 16; ++r) pmax = fmaxf(pmax, p0[r]);
  for (int r = 0; r < 16; ++r) pmax = fmaxf(pmax, p1[r]);
  { auto rr = __builtin_amdgcn_permlane32_swap(__float_as_uint(pmax), __float_as_uint(pmax), false, false);
    pmax = fmaxf(__uint_as_float(rr[0]), __uint_as_float(rr[1])); }
  if (__builtin_expect(__all(pmax - m_reg <= THR / SCALE), 1)) { mn = m_reg; alpha = 1.f; }
  else { mn = fmaxf(m_reg, pmax); alpha = __builtin_amdgcn_exp2f((m_reg - mn) * C); m_reg = mn; }
  float mnC = -mn * C;
  for (int r = 0; r < 16; ++r) p0[r] = fmaf(p0[r], C, mnC);
  for (int r = 0; r < 16; ++r) p1[r] = fmaf(p1[r], C, mnC);
  for (int r = 0; r < 16; ++r) p0[r] = __builtin_amdgcn_exp2f(p0[r]);
}
__device__ __forceinline__ void finishSM(f32x16& p0, f32x16& p1, float alpha, float& l_reg, bf16x8& pa0, bf16x8& pa1,
                                         bf16x8& pa2, bf16x8& pa3) {
  for (int r = 0; r < 16; ++r) p1[r] = __builtin_amdgcn_exp2f(p1[r]);
  float ps = 0;
  for (int r = 0; r < 16; ++r) ps += p0[r];
  for (int r = 0; r < 16; ++r) ps += p1[r];
  { auto rr = __builtin_amdgcn_permlane32_swap(__float_as_uint(ps), __float_as_uint(ps), false, false);
    ps = __uint_as_float(rr[0]) + __uint_as_float(rr[1]); }
  l_reg = l_reg * alpha + ps;
#define PK4(P, BASE, OUT) do { unsigned a0 = cvtpk(P[BASE + 0], P[BASE + 1]), a1 = cvtpk(P[BASE + 2], P[BASE + 3]);   \
    unsigned b0 = cvtpk(P[BASE + 4], P[BASE + 5]), b1 = cvtpk(P[BASE + 6], P[BASE + 7]);                              \
    auto r0 = __builtin_amdgcn_permlane32_swap(a0, b0, false, false); auto r1 = __builtin_amdgcn_permlane32_swap(a1, b1, false, false); \
    u32x4 w = {r0[0], r1[0], r0[1], r1[1]}; OUT = *reinterpret_cast<bf16x8*>(&w); } while (0)
  PK4(p0, 0, pa0); PK4(p0, 8, pa1); PK4(p1, 0, pa2); PK4(p1, 8, pa3);
#undef PK4
}
__device__ __forceinline__ void qkt(f32x16& p0, f32x16& p1, const char* Ks, const bf16x8* qr, int r32, int hi) {
  p0 = f32x16{}; p1 = f32x16{};
  for (int d0 = 0; d0 < 8; ++d0) { int cb = (d0 * 16 + hi * 8) * 2;
    bf16x8 b0 = *reinterpret_cast<const bf16x8*>(Ks + KSWZ(r32, cb));
    bf16x8 b1 = *reinterpret_cast<const bf16x8*>(Ks + KSWZ(32 + r32, cb));
    p0 = __builtin_amdgcn_mfma_f32_32x32x16_bf16(b0, qr[d0], p0, 0, 0, 0);
    p1 = __builtin_amdgcn_mfma_f32_32x32x16_bf16(b1, qr[d0], p1, 0, 0, 0); }
}
template <int D0>
__device__ __forceinline__ void pv_one(f32x16& od, int vb, bf16x8 pa0, bf16x8 pa1, bf16x8 pa2, bf16x8 pa3) {
  const s16x4 l0 = tr_read<v_rd_off(D0, 0, 0)>(vb), h0 = tr_read<v_rd_off(D0, 0, 1)>(vb), l1 = tr_read<v_rd_off(D0, 1, 0)>(vb), h1 = tr_read<v_rd_off(D0, 1, 1)>(vb);
  const s16x4 l2 = tr_read<v_rd_off(D0, 2, 0)>(vb), h2 = tr_read<v_rd_off(D0, 2, 1)>(vb), l3 = tr_read<v_rd_off(D0, 3, 0)>(vb), h3 = tr_read<v_rd_off(D0, 3, 1)>(vb);
  asm volatile("s_waitcnt lgkmcnt(0)" ::: "memory"); SBAR();
#define PK(L, H) (bf16x8){L[0], L[1], L[2], L[3], H[0], H[1], H[2], H[3]}
  od = __builtin_amdgcn_mfma_f32_32x32x16_bf16(pa0, PK(l0, h0), od, 0, 0, 0);
  od = __builtin_amdgcn_mfma_f32_32x32x16_bf16(pa1, PK(l1, h1), od, 0, 0, 0);
  od = __builtin_amdgcn_mfma_f32_32x32x16_bf16(pa2, PK(l2, h2), od, 0, 0, 0);
  od = __builtin_amdgcn_mfma_f32_32x32x16_bf16(pa3, PK(l3, h3), od, 0, 0, 0);
#undef PK
}
__device__ __forceinline__ void pv_d0(f32x16* o, int vb, bf16x8 pa0, bf16x8 pa1, bf16x8 pa2, bf16x8 pa3) {
  pv_one<0>(o[0], vb, pa0, pa1, pa2, pa3); pv_one<1>(o[1], vb, pa0, pa1, pa2, pa3);
  pv_one<2>(o[2], vb, pa0, pa1, pa2, pa3); pv_one<3>(o[3], vb, pa0, pa1, pa2, pa3);
}

template <int KS>
__device__ __forceinline__ void qproj_step(f32x16* qa, int vb, bf16x8 hb) {
  const s16x4 l0 = tr_read<v_rd_off(0, KS, 0)>(vb), h0 = tr_read<v_rd_off(0, KS, 1)>(vb);
  const s16x4 l1 = tr_read<v_rd_off(1, KS, 0)>(vb), h1 = tr_read<v_rd_off(1, KS, 1)>(vb);
  const s16x4 l2 = tr_read<v_rd_off(2, KS, 0)>(vb), h2 = tr_read<v_rd_off(2, KS, 1)>(vb);
  const s16x4 l3 = tr_read<v_rd_off(3, KS, 0)>(vb), h3 = tr_read<v_rd_off(3, KS, 1)>(vb);
  asm volatile("s_waitcnt lgkmcnt(0)" ::: "memory"); SBAR();
#define PK(L, H) (bf16x8){L[0], L[1], L[2], L[3], H[0], H[1], H[2], H[3]}
  qa[0] = __builtin_amdgcn_mfma_f32_32x32x16_bf16(PK(l0, h0), hb, qa[0], 0, 0, 0);
  qa[1] = __builtin_amdgcn_mfma_f32_32x32x16_bf16(PK(l1, h1), hb, qa[1], 0, 0, 0);
  qa[2] = __builtin_amdgcn_mfma_f32_32x32x16_bf16(PK(l2, h2), hb, qa[2], 0, 0, 0);
  qa[3] = __builtin_amdgcn_mfma_f32_32x32x16_bf16(PK(l3, h3), hb, qa[3], 0, 0, 0);
#undef PK
}

__global__ __launch_bounds__(64 * NW, 1) void k_attn(const u16* __restrict__ X, const float* __restrict__ mod,
                                                     const u16* __restrict__ Wq, const u16* __restrict__ Kc,
                                                     const u16* __restrict__ Vc, u16* __restrict__ O) {
  extern __shared__ __attribute__((aligned(16))) char lds[];
  const int bid = blockIdx.x;
  const int h = (bid & 7) + 8 * (bid >> 6);
  const long i0 = (long)((bid >> 3) & 7) * (QBLK * NW);
  const u16* Kh = Kc + (long)h * SK * HD;
  const u16* Vh = Vc + (long)h * SK * HD;
  u16* Ob = O + i0 * LDO + h * HD;
  const int tid = threadIdx.x, wid = tid >> 6, lane = tid & 63, r32 = lane & 31, hi = lane >> 5;
  char* V_lds = lds; char* K_lds = lds + 2 * SHM_V;
  float* ws = (float*)(lds + 8 * 16384) + wid * 64; float* li_l = ws; float* al_l = ws + 32;
  float m_reg = -1e30f, l_reg = 0; f32x16 o[4] = {}; bf16x8 qr[8];
  {
    const u16* xrow = X + (i0 + wid * QBLK + r32) * (long)DM + hi * 8;
    const u16* wqh = Wq + h * HD;
#pragma unroll
    for (int i = 0; i < 16; ++i) {
      const int pz = (i * NW + wid) * 1024 + lane * 16, t = pz >> 14, qq = pz & 16383;
      const int st = qq >> 9, rin = (qq >> 6) & 7, cch = (qq >> 4) & 3;
      const int kk = (st >> 2) * 8 + rin, c = (st & 3) * 32 + cch * 8;
      const int k = t * 64 + ((kk & ~0xC) | ((kk & 4) << 1) | ((kk & 8) >> 1));
      __builtin_amdgcn_global_load_lds((const unsigned*)(wqh + (long)k * (NH * HD) + c), (unsigned*)(lds + (i * NW + wid) * 1024), 16, 0, 0);
    }
    float* modl = (float*)(lds + 8 * 16384 + NW * 64 * 4);
    modl[tid] = mod[tid];
    modl[DM + tid] = rbf(1.f + mod[DM + tid]);
    bf16x8 xr[32];
#pragma unroll
    for (int ks = 0; ks < 32; ++ks) xr[ks] = *reinterpret_cast<const bf16x8*>(xrow + ks * 16);
    asm volatile("s_waitcnt vmcnt(0)" ::: "memory");
    __syncthreads();
    f32x16 qa[4] = {};
    const int vbq = (int)(uintptr_t)lds + v_rd_base(lane);
#pragma unroll
    for (int t = 0; t < 8; ++t) {
      bf16x8 hb[4];
#pragma unroll
      for (int i = 0; i < 4; ++i) {
        const int ks = t * 4 + i, c0 = ks * 16 + hi * 8;
        const f32x4 sa0 = *reinterpret_cast<const f32x4*>(modl + c0), sa1 = *reinterpret_cast<const f32x4*>(modl + c0 + 4);
        const f32x4 ca0 = *reinterpret_cast<const f32x4*>(modl + DM + c0), ca1 = *reinterpret_cast<const f32x4*>(modl + DM + c0 + 4);
        float hv[8];
#pragma unroll
        for (int e = 0; e < 8; ++e) {
          const float sa = e < 4 ? sa0[e & 3] : sa1[e & 3], ca = e < 4 ? ca0[e & 3] : ca1[e & 3];
          hv[e] = rbf(bf2f((u16)xr[ks][e]) * ca) + sa;
        }
        u32x4 w = {cvtpk(hv[0], hv[1]), cvtpk(hv[2], hv[3]), cvtpk(hv[4], hv[5]), cvtpk(hv[6], hv[7])};
        hb[i] = *reinterpret_cast<bf16x8*>(&w);
      }
      qproj_step<0>(qa, vbq + t * 16384, hb[0]); qproj_step<1>(qa, vbq + t * 16384, hb[1]);
      qproj_step<2>(qa, vbq + t * 16384, hb[2]); qproj_step<3>(qa, vbq + t * 16384, hb[3]);
      SBAR();
    }
    __syncthreads();
#define PK4Q(P, BASE, OUT) do { unsigned a0 = cvtpk(P[BASE + 0], P[BASE + 1]), a1 = cvtpk(P[BASE + 2], P[BASE + 3]);   \
    unsigned b0 = cvtpk(P[BASE + 4], P[BASE + 5]), b1 = cvtpk(P[BASE + 6], P[BASE + 7]);                              \
    auto r0 = __builtin_amdgcn_permlane32_swap(a0, b0, false, false); auto r1 = __builtin_amdgcn_permlane32_swap(a1, b1, false, false); \
    u32x4 w = {r0[0], r1[0], r0[1], r1[1]}; OUT = *reinterpret_cast<bf16x8*>(&w); } while (0)
    PK4Q(qa[0], 0, qr[0]); PK4Q(qa[0], 8, qr[1]); PK4Q(qa[1], 0, qr[2]); PK4Q(qa[1], 8, qr[3]);
    PK4Q(qa[2], 0, qr[4]); PK4Q(qa[2], 8, qr[5]); PK4Q(qa[3], 0, qr[6]); PK4Q(qa[3], 8, qr[7]);
#undef PK4Q
  }
  const int sr = tid >> 4, sc = (tid & 15) * 8, vst0 = v_st(sr, sc), vst1 = v_st(32 + sr, sc);
  const int vb0 = (int)(uintptr_t)V_lds + v_rd_base(lane);
  struct { bf16x8 vs0, vs1, ks0, ks1; } sr_[2];
#define LD8(p) (*reinterpret_cast<const bf16x8*>(p))
#define SLOAD(i, k0) do { sr_[i].vs0 = LD8(&Vh[(long)((k0) + sr) * LDK + sc]); sr_[i].vs1 = LD8(&Vh[(long)((k0) + 32 + sr) * LDK + sc]); \
    sr_[i].ks0 = LD8(&Kh[(long)((k0) + sr) * LDK + sc]); sr_[i].ks1 = LD8(&Kh[(long)((k0) + 32 + sr) * LDK + sc]); } while (0)
#define SWRITE(b, i) do { *(bf16x8*)(V_lds + (b) * SHM_V + vst0) = sr_[i].vs0;          \
    *(bf16x8*)(V_lds + (b) * SHM_V + vst1) = sr_[i].vs1; int kc = sc * 2;               \
    *(bf16x8*)(K_lds + (b) * SHM_K + KSWZ(sr, kc)) = sr_[i].ks0;                       \
    *(bf16x8*)(K_lds + (b) * SHM_K + KSWZ(32 + sr, kc)) = sr_[i].ks1; } while (0)
#define SWAIT() asm volatile("s_waitcnt vmcnt(4)" ::: "memory")
#define RESC(a) do { if (__any((a) < 1.f)) { if (hi == 0) al_l[r32] = (a); asm volatile("s_waitcnt lgkmcnt(0)" ::: "memory"); \
    for (int d = 0; d < 4; ++d) for (int r = 0; r < 16; ++r) o[d][r] *= al_l[crow(r, hi)]; } } while (0)
  f32x16 pA0, pA1, pB0, pB1; float mnA, mnB, alA, alB; bf16x8 pa0, pa1, pa2, pa3; const int NT = SK / KVBLK;
  constexpr int SE = 0, SO = 1;
  SLOAD(SE, 0); asm volatile("s_waitcnt vmcnt(0)" ::: "memory"); SWRITE(0, SE); __syncthreads();
  qkt(pA0, pA1, K_lds, qr, r32, hi); partialSM(pA0, pA1, m_reg, mnA, alA);
  SLOAD(SO, KVBLK); SLOAD(SE, 2 * KVBLK);
  SWAIT(); SWRITE(1, SO); __syncthreads();
  for (int j = 1; j + 1 < NT; j += 2) {
    SBAR(); qkt(pB0, pB1, K_lds + SHM_K, qr, r32, hi);
    finishSM(pA0, pA1, alA, l_reg, pa0, pa1, pa2, pa3); SBAR();
    SLOAD(SO, (j + 2) * KVBLK); SBAR();
    pv_d0(o, vb0, pa0, pa1, pa2, pa3); partialSM(pB0, pB1, m_reg, mnB, alB);
    __syncthreads(); SWAIT(); SWRITE(0, SE);
    RESC(alB); __syncthreads();
    SBAR(); qkt(pA0, pA1, K_lds, qr, r32, hi);
    finishSM(pB0, pB1, alB, l_reg, pa0, pa1, pa2, pa3); SBAR();
    if (j + 3 < NT) SLOAD(SE, (j + 3) * KVBLK); SBAR();
    pv_d0(o, vb0 + (int)SHM_V, pa0, pa1, pa2, pa3); partialSM(pA0, pA1, m_reg, mnA, alA);
    __syncthreads(); SWAIT(); SWRITE(1, SO);
    RESC(alA); __syncthreads();
  }
  SBAR(); qkt(pB0, pB1, K_lds + SHM_K, qr, r32, hi);
  finishSM(pA0, pA1, alA, l_reg, pa0, pa1, pa2, pa3); SBAR();
  pv_d0(o, vb0, pa0, pa1, pa2, pa3); partialSM(pB0, pB1, m_reg, mnB, alB);
  __syncthreads(); RESC(alB);
  finishSM(pB0, pB1, alB, l_reg, pa0, pa1, pa2, pa3); SBAR();
  pv_d0(o, vb0 + (int)SHM_V, pa0, pa1, pa2, pa3);
  if (hi == 0) li_l[r32] = l_reg;
  asm volatile("s_waitcnt lgkmcnt(0)" ::: "memory");
  float rli[16];
#pragma unroll
  for (int r = 0; r < 16; ++r) rli[r] = __builtin_amdgcn_rcpf(li_l[crow(r, hi)]);
  u16* Ow = Ob + (long)(wid * QBLK) * LDO;
#pragma unroll
  for (int r = 0; r < 16; ++r) { int orow = crow(r, hi);
    for (int d0 = 0; d0 < 4; ++d0) Ow[(long)orow * LDO + d0 * 32 + r32] = f2bf(o[d0][r] * rli[r]); }
#undef SLOAD
#undef SWRITE
#undef SWAIT
#undef RESC
#undef LD8
}

extern "C" void kernel_launch(void* const* d_in, const int* in_sizes, int n_in, void* d_out, int out_size, void* d_ws,
                              size_t ws_size, hipStream_t stream) {
  const u16* x = (const u16*)d_in[0];
  const u16* kc = (const u16*)d_in[1];
  const u16* vc = (const u16*)d_in[2];
  const u16* wq = (const u16*)d_in[3];
  const u16* wo = (const u16*)d_in[4];
  const u16* w_in = (const u16*)d_in[6];
  const u16* w_sp = (const u16*)d_in[7];
  const u16* b_sp = (const u16*)d_in[8];
  const u16* w_out = (const u16*)d_in[9];
  const u16* cvec = (const u16*)d_in[10];
  const u16* w_mod = (const u16*)d_in[11];
  u16* out = (u16*)d_out;

  char* ws = (char*)d_ws;
  size_t off = 0;
  auto alloc = [&](size_t bytes) { char* p = ws + off; off += (bytes + 255) & ~(size_t)255; return p; };
  float* mod = (float*)alloc(6 * DM * 4);
  u16* h1 = (u16*)alloc((size_t)SEQ * DM * 2);
  u16* q = (u16*)alloc((size_t)SEQ * NH * HD * 2);
  u16* o = (u16*)alloc((size_t)SEQ * NH * HD * 2);
  float* part = (float*)alloc((size_t)SPLITA * SEQ * DM * 4);
  u16* x1 = (u16*)alloc((size_t)SEQ * DM * 2);
  u16* h2 = (u16*)alloc((size_t)SEQ * DM * 2);
  u16* z1 = (u16*)alloc((size_t)SEQ * FH * 2);
  u16* z2 = (u16*)alloc((size_t)SEQ * FH * 2);
  u16* z2n = (u16*)alloc((size_t)SEQ * FH * 2);
  u16* u = (u16*)alloc((size_t)SEQ * FH * 2);

  (void)hipFuncSetAttribute((const void*)k_attn, hipFuncAttributeMaxDynamicSharedMemorySize, (int)SHM_ATTN);

  k_mod<<<dim3(6 * DM / 64 + SEQ / 4), dim3(256), 0, stream>>>(cvec, w_mod, mod, x, h1);
  k_attn<<<dim3(256), dim3(512), SHM_ATTN, stream>>>(h1, mod, wq, kc, vc, o);
  {
    GemmArgs g{o, wo, part, nullptr, nullptr, nullptr, nullptr, NH * HD, DM, DM, NH * HD / SPLITA};
    launch_gemm<64, 2, 4, 5, EPI_F32PART, 1>(g, SEQ, DM, SPLITA, stream);
  }
  k_reduce_ln<<<dim3(SEQ / 4), dim3(256), 0, stream>>>(part, x, mod, x1, h2);
  {
    GemmArgs g{h2, w_in, z1, z2, nullptr, nullptr, nullptr, DM, FF, FH, DM};
    launch_gemm<128, 2, 4, 4, EPI_GELU, 0>(g, SEQ, FF, 1, stream);
  }
  k_ln_z2<<<dim3(SEQ / 4), dim3(256), 0, stream>>>(z2, z2n);
  {
    GemmArgs g{w_sp, z2n, u, nullptr, b_sp, z1, nullptr, SEQ, FH, FH, SEQ};
    launch_gemm<64, 2, 4, 5, EPI_GATE, 0>(g, SEQ, FH, 1, stream);
  }
  {
    GemmArgs g{u, w_out, out, nullptr, x1, nullptr, mod, FH, DM, DM, FH};
    launch_gemm<32, 1, 4, 4, EPI_FINAL, 0>(g, SEQ, DM, 1, stream);
  }
}
```
